```python
import torch

def _softmax(x, axis=-1):
    return torch.softmax(x, dim=axis)
def _gelu(x):
    return torch.nn.functional.gelu(x, approximate="tanh")
def _silu(x):
    return torch.nn.functional.silu(x)
def _sigmoid(x):
    return torch.sigmoid(x)
def _layernorm(x, axis=-1, eps=1e-5):
    m = x.mean(dim=axis, keepdim=True)
    v = x.var(dim=axis, keepdim=True, unbiased=False)
    return (x - m) / torch.sqrt(v + eps)
def _scatter_add(tgt, idx, src, axis=0):
    return tgt.clone().index_add_(axis, idx, src)
def _segment_sum(src, seg, n):
    out = torch.zeros((n,) + src.shape[1:], dtype=src.dtype)
    return out.index_add_(0, seg, src)
def _pad(x, pw):
    flat = tuple(v for pair in reversed(pw) for v in pair)
    return torch.nn.functional.pad(x, flat)

kernel_name = 'recurrent_scan__ssm_conv_h3__mixer_s_mlp__prompt'

def setup_inputs():

    _w = (lambda a: (lambda t: t.to(torch.bfloat16) if t.is_floating_point() else t)(torch.as_tensor(a)))
    def _A_setup():
        B, S, D = 1, 2048, 1024
        ks = 4
        return {
            "q": _w((lambda *s: torch.randn(*s, dtype=torch.bfloat16) * 0.05)(B, S, D)),
            "k": _w((lambda *s: torch.randn(*s, dtype=torch.bfloat16) * 0.2)(B, S, D)),
            "v": _w((lambda *s: torch.randn(*s, dtype=torch.bfloat16) * 0.2)(B, S, D)),
            "shift_k": _w((lambda *s: torch.randn(*s, dtype=torch.bfloat16) * 0.2)(D, ks) * 0.5),
            "ssm_k": _w((lambda *s: torch.randn(*s, dtype=torch.bfloat16) * 0.2)(D, S) * (1.0 / S ** 0.5)),
            "ssm_D": _w((lambda *s: torch.randn(*s, dtype=torch.bfloat16) * 0.2)(D)),
        }
    _A_in = _A_setup()

    def _B_setup():
        B, S, D, F = 1, 2048, 1024, 3584
        return {
            "x":  _w((lambda *s: torch.randn(*s, dtype=torch.bfloat16) * 0.05)(B, S, D)),
            "w1": _w((lambda *s: torch.randn(*s, dtype=torch.bfloat16) * 1.6)(D, D) * (D ** -0.5)),
            "w2": _w((lambda *s: torch.randn(*s, dtype=torch.bfloat16) * 1.6)(D, D) * (D ** -0.5)),
            "w3": _w((lambda *s: torch.randn(*s, dtype=torch.bfloat16) * 1.6)(D, F) * (D ** -0.5)),
            "w4": _w((lambda *s: torch.randn(*s, dtype=torch.bfloat16) * 1.6)(F, D) * (F ** -0.5)),
        }
    _B_in = _B_setup()

    return {
        "A_q": _A_in["q"],
        "A_k": _A_in["k"],
        "A_v": _A_in["v"],
        "A_shift_k": _A_in["shift_k"],
        "A_ssm_k": _A_in["ssm_k"],
        "A_ssm_D": _A_in["ssm_D"],
        "B_x": _B_in["x"],
        "B_w1": _B_in["w1"],
        "B_w2": _B_in["w2"],
        "B_w3": _B_in["w3"],
        "B_w4": _B_in["w4"],
    }

def reference(A_q, A_k, A_v, A_shift_k, A_ssm_k, A_ssm_D, B_x, B_w1, B_w2, B_w3, B_w4):
    def _A_ref(q, k, v, shift_k, ssm_k, ssm_D):
        B, S, D = q.shape
        ks = shift_k.shape[-1]
        k_pad = _pad(k, ((0, 0), (ks - 1, 0), (0, 0)))
        k_shift = torch.zeros_like(k)
        for t in range(ks):
            k_shift = k_shift + k_pad[:, t:t + S, :] * shift_k[:, ks - 1 - t][None, None, :]
        kv = k_shift * v
        L = 2 * S
        kv_pad = _pad(kv, ((0, 0), (0, S), (0, 0)))
        kf_pad = _pad(ssm_k, ((0, 0), (0, S)))
        kv_f = (lambda x, axes=(-2,-1): torch.fft.rfft2(x.float(), dim=axes))(kv_pad[:, None, :, :], axes=(1, 2))
        kf_f = (lambda x, axes=(-2,-1): torch.fft.rfft2(x.float(), dim=axes))(kf_pad[:, None, :], axes=(1, 2))
        kf_f = (lambda x, *d: x.permute(*d))(kf_f, 1, 2, 0)
        y_f = kv_f * kf_f[None, :, :, :]
        y = (lambda x, s, axes=(-2,-1): torch.fft.irfft2(x, s=s, dim=axes).to(torch.bfloat16))(y_f, (1, L), axes=(1, 2))[:, 0, :S, :]
        y = y + kv * ssm_D[None, None, :]
        return q * y

    def _B_ref(x, w1, w2, w3, w4):
        B, S, D = x.shape
        h = _gelu(_layernorm(x, axis=-1) @ w1)
        c = D // 4
        g0 = _pad(h[:, :, 0*c:1*c], ((0, 0), (1, 0), (0, 0)))[:, :S, :]
        g1 = _pad(h[:, :, 1*c:2*c], ((0, 0), (0, 1), (0, 0)))[:, 1:, :]
        g2 = _pad(h[:, :, 2*c:3*c], ((0, 0), (2, 0), (0, 0)))[:, :S, :]
        g3 = _pad(h[:, :, 3*c:   ], ((0, 0), (0, 2), (0, 0)))[:, 2:, :]
        hs = torch.concatenate([g0, g1, g2, g3], axis=-1) @ w2
        x1 = x + hs
        h2 = _gelu(_layernorm(x1, axis=-1) @ w3) @ w4
        return x1 + h2

    x = A_q
    xn = _layernorm(x, axis=-1)
    s = _A_ref(xn, A_k, A_v, A_shift_k, A_ssm_k, A_ssm_D)
    g = _silu(_B_ref(xn, B_w1, B_w2, B_w3, B_w4))
    return x + s * g
```

```cpp
#include <hip/hip_runtime.h>
#include <hip/hip_bf16.h>
#include <stdint.h>

typedef unsigned short u16;
typedef unsigned int u32;
typedef __attribute__((ext_vector_type(8))) __bf16 bf16x8;
typedef __attribute__((ext_vector_type(4))) short s16x4;
typedef __attribute__((ext_vector_type(8))) short s16x8;
typedef __attribute__((ext_vector_type(4))) float f32x4;
typedef __attribute__((ext_vector_type(2))) float f32x2;
typedef __attribute__((ext_vector_type(2))) u32 u32x2;
typedef __attribute__((ext_vector_type(4))) u32 u32x4;
typedef __attribute__((ext_vector_type(2))) __bf16 bf16x2;
typedef __attribute__((address_space(3))) void* ldsptr_t;
typedef __attribute__((address_space(3))) s16x4* lds_s16x4_t;

constexpr int S = 2048, D = 1024, F = 3584;
constexpr int URS = 2304;

constexpr size_t WS_XN = 0;
constexpr size_t WS_A1 = WS_XN + (size_t)S * D * 2;
constexpr size_t WS_H = WS_A1 + (size_t)S * D * 2;
constexpr size_t WS_X1 = WS_H + (size_t)(S + 4) * D * 2;
constexpr size_t WS_G3 = WS_X1 + (size_t)S * D * 2;
constexpr size_t WS_UR = WS_G3 + (size_t)S * F * 2;
constexpr size_t WS_YT = WS_UR + (size_t)D * URS * 2 + 4096;
constexpr size_t WS_ST = WS_YT + (size_t)S * D * 2;
constexpr size_t WS_CS = WS_ST + (size_t)S * 16 * 8;
constexpr size_t WS_END = WS_CS + (size_t)16 * F * 4;

extern __shared__ __attribute__((aligned(16))) char smem[];

__device__ __forceinline__ float bf_lo(u32 v) { return __uint_as_float(v << 16); }
__device__ __forceinline__ float bf_hi(u32 v) { return __uint_as_float(v & 0xffff0000u); }
__device__ __forceinline__ u32 pack2(float lo, float hi) {
  bf16x2 r;
  r[0] = (__bf16)lo;
  r[1] = (__bf16)hi;
  return __builtin_bit_cast(u32, r);
}
__device__ __forceinline__ float rbf(float x) { return (float)(__bf16)x; }

__device__ __forceinline__ float wave_sum(float v) {
#pragma unroll
  for (int o = 32; o > 0; o >>= 1) v += __shfl_xor(v, o, 64);
  return v;
}

#define WAIT_VM(n) asm volatile("s_waitcnt vmcnt(%0)" ::"n"(n) : "memory")
#define WAIT_LGKM0() asm volatile("s_waitcnt lgkmcnt(0)" ::: "memory")

__device__ __forceinline__ __amdgpu_buffer_rsrc_t mk_rsrc(const void* p, u32 bytes) {
  return __builtin_amdgcn_make_buffer_rsrc((void*)p, 0, bytes, 0x00020000);
}
__device__ __forceinline__ void glds16(__amdgpu_buffer_rsrc_t r, u32 lds_off, u32 voff, u32 soff) {
  asm volatile("s_mov_b32 m0, %0\n\ts_nop 0\n\tbuffer_load_dwordx4 %1, %2, %3 offen lds" ::"s"(lds_off), "v"(voff), "s"(r),
               "s"(soff)
               : "memory");
}

constexpr int PRE_KV_WG = 256, PRE_LN_WG = 512, PRE_CS_WG = 112;

__device__ __forceinline__ void ln_row16(const float (&x)[16], float (&y)[16]) {
  float s = 0.f;
#pragma unroll
  for (int i = 0; i < 16; ++i) s += x[i];
  const float mean = wave_sum(s) * (1.0f / D);
  float q = 0.f;
#pragma unroll
  for (int i = 0; i < 16; ++i) q += (x[i] - mean) * (x[i] - mean);
  const float var = wave_sum(q) * (1.0f / D);
  const float m = rbf(mean);
  const float sd = rbf(sqrtf(rbf(rbf(var) + 1e-5f)));
  const float rs = 1.0f / sd;
#pragma unroll
  for (int i = 0; i < 16; ++i) y[i] = rbf(rbf(x[i] - m) * rs);
}

__global__ __launch_bounds__(256) void pre_kernel(const u16* __restrict__ xq, const u16* __restrict__ kk,
                                                  const u16* __restrict__ vv, const u16* __restrict__ shk,
                                                  const u16* __restrict__ w3, char* __restrict__ ws) {
  const int tid = threadIdx.x;
  int b = blockIdx.x;
  if (b < PRE_KV_WG) {
    const int t0 = (b >> 4) * 128, d0 = (b & 15) * 64;
    const int tr = tid >> 3, dc = tid & 7;
    const int tb = t0 + 4 * tr, dd = d0 + 8 * dc;
    u32x4 kr[7], vr[4];
#pragma unroll
    for (int i = 0; i < 7; ++i) {
      const int t = tb - 3 + i;
      const u32x4 z = {0u, 0u, 0u, 0u};
      kr[i] = (t >= 0) ? *(const u32x4*)(kk + (size_t)(t < 0 ? 0 : t) * D + dd) : z;
    }
#pragma unroll
    for (int i = 0; i < 4; ++i) vr[i] = *(const u32x4*)(vv + (size_t)(tb + i) * D + dd);
    u32x4 sh[4];
#pragma unroll
    for (int i = 0; i < 4; ++i) sh[i] = *(const u32x4*)(shk + (size_t)dd * 4 + 8 * i);
    u32* T = (u32*)smem;
#pragma unroll
    for (int e = 0; e < 8; ++e) {
      const u32 s01 = sh[e >> 1][(e & 1) * 2], s23 = sh[e >> 1][(e & 1) * 2 + 1];
      const float c0 = bf_lo(s01), c1 = bf_hi(s01), c2 = bf_lo(s23), c3 = bf_hi(s23);
      float kf[7], o[4];
#pragma unroll
      for (int i = 0; i < 7; ++i) kf[i] = (e & 1) ? bf_hi(kr[i][e >> 1]) : bf_lo(kr[i][e >> 1]);
#pragma unroll
      for (int i = 0; i < 4; ++i) {
        float a = rbf(kf[i] * c3);
        a = rbf(a + rbf(kf[i + 1] * c2));
        a = rbf(a + rbf(kf[i + 2] * c1));
        a = rbf(a + rbf(kf[i + 3] * c0));
        const float vf = (e & 1) ? bf_hi(vr[i][e >> 1]) : bf_lo(vr[i][e >> 1]);
        o[i] = a * vf;
      }
      u32x2 pk = {pack2(o[3], o[2]), pack2(o[1], o[0])};
      const int dw = 62 - 2 * tr;
      const int dws = dw ^ (4 * dc);
      *(u32x2*)(T + (8 * dc + e) * 64 + dws) = pk;
    }
    __syncthreads();
    u16* ur = (u16*)(ws + WS_UR);
    const int x0 = S - 128 - t0;
#pragma unroll
    for (int i = 0; i < 4; ++i) {
      const int c = tid & 15, d = (tid >> 4) + 16 * i;
      const u32x4 val = *(const u32x4*)(T + d * 64 + ((4 * c) ^ (4 * (d >> 3))));
      *(u32x4*)(ur + (size_t)(d0 + d) * URS + x0 + 8 * c) = val;
    }
    if (t0 == 0) {
      const u32x4 z = {0u, 0u, 0u, 0u};
#pragma unroll
      for (int i = 0; i < 8; ++i) {
        const int c = tid & 31, d = (tid >> 5) + 8 * i;
        *(u32x4*)(ur + (size_t)(d0 + d) * URS + S + 8 * c) = z;
      }
    }
    if (b == 255) {
      const u32x4 z = {0u, 0u, 0u, 0u};
      u16* h = (u16*)(ws + WS_H);
      *(u32x4*)(h + tid * 8) = z;
      *(u32x4*)(h + (size_t)(S + 2) * D + tid * 8) = z;
    }
    return;
  }
  b -= PRE_KV_WG;
  if (b < PRE_LN_WG) {
    const int row = b * 4 + (tid >> 6), lane = tid & 63;
    const u32x4 r0 = *(const u32x4*)(xq + (size_t)row * D + lane * 8);
    const u32x4 r1 = *(const u32x4*)(xq + (size_t)row * D + 512 + lane * 8);
    float x[16], y[16], z[16];
#pragma unroll
    for (int i = 0; i < 4; ++i) {
      x[2 * i] = bf_lo(r0[i]);
      x[2 * i + 1] = bf_hi(r0[i]);
      x[8 + 2 * i] = bf_lo(r1[i]);
      x[8 + 2 * i + 1] = bf_hi(r1[i]);
    }
    ln_row16(x, y);
    ln_row16(y, z);
    u32x4 o0, o1, p0, p1;
#pragma unroll
    for (int i = 0; i < 4; ++i) {
      o0[i] = pack2(y[2 * i], y[2 * i + 1]);
      o1[i] = pack2(y[8 + 2 * i], y[8 + 2 * i + 1]);
      p0[i] = pack2(z[2 * i], z[2 * i + 1]);
      p1[i] = pack2(z[8 + 2 * i], z[8 + 2 * i + 1]);
    }
    u16* xn = (u16*)(ws + WS_XN);
    u16* a1 = (u16*)(ws + WS_A1);
    *(u32x4*)(xn + (size_t)row * D + lane * 8) = o0;
    *(u32x4*)(xn + (size_t)row * D + 512 + lane * 8) = o1;
    *(u32x4*)(a1 + (size_t)row * D + lane * 8) = p0;
    *(u32x4*)(a1 + (size_t)row * D + 512 + lane * 8) = p1;
    return;
  }
  b -= PRE_LN_WG;
  {
    const int rb = b / 7, cb = b % 7;
    const int w = tid >> 6, lane = tid & 63;
    const u16* p = w3 + (size_t)(rb * 64 + w * 16) * F + cb * 512 + lane * 8;
    u32x4 r[16];
#pragma unroll
    for (int i = 0; i < 16; ++i) r[i] = *(const u32x4*)(p + (size_t)i * F);
    float s[8];
#pragma unroll
    for (int e = 0; e < 8; ++e) s[e] = 0.f;
#pragma unroll
    for (int i = 0; i < 16; ++i)
#pragma unroll
      for (int e = 0; e < 4; ++e) {
        s[2 * e] += bf_lo(r[i][e]);
        s[2 * e + 1] += bf_hi(r[i][e]);
      }
    float* T = (float*)smem;
    *(f32x4*)(T + w * 512 + lane * 8) = f32x4{s[0], s[1], s[2], s[3]};
    *(f32x4*)(T + w * 512 + lane * 8 + 4) = f32x4{s[4], s[5], s[6], s[7]};
    __syncthreads();
    float* cs = (float*)(ws + WS_CS);
    const f32x2 a = *(const f32x2*)(T + 2 * tid), c = *(const f32x2*)(T + 512 + 2 * tid);
    const f32x2 e = *(const f32x2*)(T + 1024 + 2 * tid), g = *(const f32x2*)(T + 1536 + 2 * tid);
    *(f32x2*)(cs + (size_t)rb * F + cb * 512 + 2 * tid) = (a + c) + (e + g);
  }
}

constexpr int CV_U = 4640, CV_H = 4224, CV_CH = 4 * CV_U + 2 * CV_H;

__device__ __forceinline__ bf16x8 lds_rd16(u32 off) { return *(const bf16x8*)(smem + off); }

__global__ __launch_bounds__(256) void conv_kernel(const u16* __restrict__ ssm_k, const u16* __restrict__ ssm_D,
                                                   char* __restrict__ ws) {
  const int lane = threadIdx.x & 63;
  const int w = __builtin_amdgcn_readfirstlane(threadIdx.x >> 6);
  const int ch = blockIdx.x * 4 + w;
  const u32 base = w * CV_CH, hb = base + 4 * CV_U;
  const __amdgpu_buffer_rsrc_t rU = mk_rsrc(ws + WS_UR, D * URS * 2 + 4096);
  const __amdgpu_buffer_rsrc_t rH = mk_rsrc(ssm_k, D * S * 2);
  const u16* hrow = ssm_k + (size_t)ch * S;
  u32x4 hc[4];
  u32 hp[4];
#pragma unroll
  for (int i = 0; i < 4; ++i) {
    const int c = i * 64 + lane;
    hc[i] = *(const u32x4*)(hrow + 8 * c);
    hp[i] = c > 0 ? *(const u32*)(hrow + (c > 0 ? 8 * c - 2 : 0)) : 0u;
  }
  const float dcoef = bf_lo((u32)ssm_D[ch]);
#pragma unroll
  for (int c = 0; c < 4; ++c) {
#pragma unroll
    for (int i = 0; i < 4; ++i) glds16(rU, base + c * CV_U + i * 1024, lane * 16, ch * (URS * 2) + i * 1024 + (12 - 4 * c));
    if (lane < 32) glds16(rU, base + c * CV_U + 4096, lane * 16, ch * (URS * 2) + 4096 + (12 - 4 * c));
  }
#pragma unroll
  for (int i = 0; i < 4; ++i) glds16(rH, hb + CV_H + 112 + i * 1024, lane * 16, ch * (S * 2) + i * 1024);
  {
    const u32x4 z = {0u, 0u, 0u, 0u};
    if (lane < 7) *(u32x4*)(smem + hb + lane * 16) = z;
    else if (lane < 14) *(u32x4*)(smem + hb + CV_H + (lane - 7) * 16) = z;
  }
#pragma unroll
  for (int i = 0; i < 4; ++i) {
    u32x4 o;
    o[0] = __builtin_amdgcn_alignbit(hc[i][0], hp[i], 16);
    o[1] = __builtin_amdgcn_alignbit(hc[i][1], hc[i][0], 16);
    o[2] = __builtin_amdgcn_alignbit(hc[i][2], hc[i][1], 16);
    o[3] = __builtin_amdgcn_alignbit(hc[i][3], hc[i][2], 16);
    *(u32x4*)(smem + hb + 112 + (i * 64 + lane) * 16) = o;
  }
  WAIT_VM(0);
  WAIT_LGKM0();
  const int r16 = lane & 15, g = lane >> 4;
  const u32 aaddr = hb + (r16 & 1) * CV_H + ((r16 >> 1) + g) * 16;
  const u32 baddr = base + (r16 & 3) * CV_U + (248 - 8 * (r16 >> 2) + g) * 16;
  f32x4 acc[8];
#pragma unroll
  for (int b = 0; b < 8; ++b) acc[b] = f32x4{0.f, 0.f, 0.f, 0.f};
#pragma unroll 1
  for (int r = 0; r < 8; ++r) {
    bf16x8 A[8], B[8];
    const u32 a0 = aaddr + 64 * r, b0 = baddr + 64 * r - 7 * 512;
#pragma unroll
    for (int p = 0; p < 8; ++p) {
      A[p] = lds_rd16(a0 + 512 * p);
      B[p] = lds_rd16(b0 + 512 * (7 - p));
    }
#pragma unroll
    for (int p = 0; p < 8; ++p)
#pragma unroll
      for (int q = 0; q + p < 8; ++q)
        acc[p + q] = __builtin_amdgcn_mfma_f32_16x16x32_bf16(A[p], B[q], acc[p + q], 0, 0, 0);
  }
#pragma unroll
  for (int b = 0; b < 8; ++b) {
    const int t = 256 * b + 16 * g + 2 * (r16 & 3) + 64 * (r16 >> 2);
    *(u32*)(smem + hb + t * 2) = pack2(acc[b][0], acc[b][1]);
    *(u32*)(smem + hb + t * 2 + 16) = pack2(acc[b][2], acc[b][3]);
  }
  u16* yt = (u16*)(ws + WS_YT) + (size_t)ch * S;
#pragma unroll
  for (int i = 0; i < 4; ++i) {
    const int c = i * 64 + lane;
    const u32x4 y = *(const u32x4*)(smem + hb + c * 16);
    const u32x4 u = *(const u32x4*)(smem + base + 3 * CV_U + (255 - c) * 16);
    u32x4 o;
#pragma unroll
    for (int e = 0; e < 4; ++e) {
      const float lo = bf_lo(y[e]) + rbf(bf_hi(u[3 - e]) * dcoef);
      const float hi = bf_hi(y[e]) + rbf(bf_lo(u[3 - e]) * dcoef);
      o[e] = pack2(lo, hi);
    }
    *(u32x4*)(yt + c * 8) = o;
  }
}

struct GemmArgs {
  const u16* A;
  const u16* W;
  const u16* x;
  char* ws;
  u16* out;
};

template <int BN>
__device__ __forceinline__ int wswz(int r) {
  if (BN == 64) return 2 * (((r >> 1) & 1) + 2 * ((r >> 3) & 1));
  if (BN == 224) return 2 * ((r >> 3) & 1);
  return 2 * ((r & 3) + 4 * ((r >> 3) & 1));
}

__device__ __forceinline__ int yswz(int r) { return 2 * ((r & 3) + 4 * ((r >> 4) & 1)); }

__device__ __forceinline__ float gelu_t(float x) {
  const float c0 = -2.302208198f, c1 = -0.1029432397f;
  const float a = x * __builtin_fmaf(x * x, c1, c0);
  return x * __builtin_amdgcn_rcpf(1.0f + __builtin_amdgcn_exp2f(a));
}

template <int EPI, int BM, int BN, int WGM, int WGN, int NS, int K, int LDA, int LDW, int PM, int PN>
__global__ __launch_bounds__(64 * WGM * WGN) void gemm_kernel(GemmArgs a) {
  constexpr int NW = WGM * WGN;
  constexpr int WTM = BM / WGM, WTN = BN / WGN, MT = WTM / 16, NT = WTN / 16;
  constexpr int WROW = BN * 2, CPR = BN / 8;
  constexpr int A_BYTES = BM * 128, W_BYTES = 64 * WROW, STAGE = A_BYTES + W_BYTES;
  constexpr int PA = (A_BYTES / 1024) / NW;
  constexpr int PW = (W_BYTES / 1024) / NW;
  constexpr bool HALF = (W_BYTES / 1024) % NW != 0;
  static_assert(!HALF || (W_BYTES / 512) % NW == 0, "pieces");
  constexpr int PER = PA + PW + (HALF ? 1 : 0);
  constexpr int KT = K / 64;
  constexpr int TN = (EPI == 3 ? F : D) / BN;
  constexpr u32 XTRA = NS * STAGE;

  const int tid = threadIdx.x, lane = tid & 63;
  const int w = __builtin_amdgcn_readfirstlane(tid >> 6);
  const int wm = w / WGN, wn = w % WGN;
  const int xcd = blockIdx.x & 7, idx = blockIdx.x >> 3;
  constexpr int XN_ = TN / PN;
  const int tm = (xcd / XN_) * PM + idx / PN, tn = (xcd % XN_) * PN + idx % PN;
  const int m0 = tm * BM, n0 = tn * BN;

  const __amdgpu_buffer_rsrc_t rA = mk_rsrc(a.A, (EPI == 2 ? S + 4 : S) * LDA * 2);
  const __amdgpu_buffer_rsrc_t rW = mk_rsrc(a.W, K * LDW * 2);
  const u32 voffA = (lane >> 3) * (LDA * 2) + (((lane & 7) ^ (lane >> 3)) * 16);
  u32 voffW[PW + 1];
#pragma unroll
  for (int i = 0; i < PW + (HALF ? 1 : 0); ++i) {
    const int L = (i < PW) ? (i * NW + w) * 64 + lane : PW * NW * 64 + w * 32 + (lane & 31);
    const int kr = L / CPR, c = (L % CPR) ^ wswz<BN>(kr);
    voffW[i] = kr * (LDW * 2) + c * 16;
  }
  auto stage = [&](int kt, int buf) {
    int arow = m0;
    if (EPI == 2) {
      const int grp = kt >> 2;
      arow += 2 + (grp == 0 ? -1 : grp == 1 ? 1 : grp == 2 ? -2 : 2);
    }
    const u32 sa = arow * (LDA * 2) + kt * 128;
    const u32 sw = kt * 64 * (LDW * 2) + n0 * 2;
    const u32 lb = buf * STAGE;
#pragma unroll
    for (int i = 0; i < PA; ++i) glds16(rA, lb + (i * NW + w) * 1024, voffA, sa + (i * NW + w) * 8 * (LDA * 2));
#pragma unroll
    for (int i = 0; i < PW; ++i) glds16(rW, lb + A_BYTES + (i * NW + w) * 1024, voffW[i], sw);
    if (HALF) {
      if (lane < 32) glds16(rW, lb + A_BYTES + PW * NW * 1024 + w * 512, voffW[PW], sw);
    }
  };

  const int r16 = lane & 15, g = lane >> 4;
  const u32 aoff = (wm * WTM + r16) * 128 + ((g ^ (lane & 7)) * 16);
  u32 woff[NT];
  {
    const int q = r16 >> 2, p = lane & 3, kr = 8 * g + q;
#pragma unroll
    for (int j = 0; j < NT; ++j)
      woff[j] = A_BYTES + kr * WROW + (((wn * (WTN / 8) + 2 * j + (p >> 1)) ^ wswz<BN>(kr)) * 16) + 8 * (p & 1);
  }

#pragma unroll
  for (int s = 0; s < NS - 1; ++s) stage(s, s);

  f32x4 acc[MT][NT];
  float rowc0[MT], rowc1[MT], rowsd[MT];
  if (EPI == 3) {
    float* tab = (float*)(smem + XTRA);
    const float* st = (const float*)(a.ws + WS_ST);
    const float* cs = (const float*)(a.ws + WS_CS);
    constexpr int TPR = 64 * NW / BM;
    {
      const int row = tid / TPR, part = tid % TPR;
      float s1 = 0.f, s2 = 0.f;
#pragma unroll
      for (int i = 0; i < 8 / TPR; ++i) {
        const f32x4 v = *(const f32x4*)(st + (size_t)(m0 + row) * 32 + (part * (8 / TPR) + i) * 4);
        s1 += v[0] + v[2];
        s2 += v[1] + v[3];
      }
#pragma unroll
      for (int o = 1; o < TPR; o <<= 1) {
        s1 += __shfl_xor(s1, o, 64);
        s2 += __shfl_xor(s2, o, 64);
      }
      const float mean = s1 * (1.0f / D);
      const float var = fmaxf(s2 * (1.0f / D) - mean * mean, 0.f);
      if (part == 0) {
        tab[2 * row] = rbf(mean);
        tab[2 * row + 1] = rbf(sqrtf(rbf(rbf(var) + 1e-5f)));
      }
    }
    if (tid < BN) {
      float c = 0.f;
#pragma unroll
      for (int i = 0; i < 16; ++i) c += cs[(size_t)i * F + n0 + tid];
      tab[2 * BM + tid] = c;
    }
    WAIT_LGKM0();
    __builtin_amdgcn_s_barrier();
#pragma unroll
    for (int i = 0; i < MT; ++i) {
      const f32x2 ms = *(const f32x2*)(tab + 2 * (wm * WTM + 16 * i + r16));
      const float rs = __builtin_amdgcn_rcpf(ms[1]);
      rowc0[i] = -2.302208198f * rs;
      rowc1[i] = -0.1029432397f * rs * rs * rs;
      rowsd[i] = ms[1];
#pragma unroll
      for (int j = 0; j < NT; ++j) {
        const f32x4 c = *(const f32x4*)(tab + 2 * BM + wn * WTN + 16 * j + 4 * g);
        acc[i][j] = c * (-ms[0]);
      }
    }
  } else {
#pragma unroll
    for (int i = 0; i < MT; ++i)
#pragma unroll
      for (int j = 0; j < NT; ++j) acc[i][j] = f32x4{0.f, 0.f, 0.f, 0.f};
  }

  auto ldfrag = [&](bf16x8 (&fa)[MT], bf16x8 (&fw)[NT], u32 lb, int kk) {
#pragma unroll
    for (int j = 0; j < NT; ++j) {
      const s16x4 lo = __builtin_amdgcn_ds_read_tr16_b64_v4i16((lds_s16x4_t)(smem + lb + woff[j] + kk * 32 * WROW));
      const s16x4 hi = __builtin_amdgcn_ds_read_tr16_b64_v4i16((lds_s16x4_t)(smem + lb + woff[j] + (kk * 32 + 4) * WROW));
      fw[j] = __builtin_bit_cast(bf16x8, __builtin_shufflevector(lo, hi, 0, 1, 2, 3, 4, 5, 6, 7));
    }
#pragma unroll
    for (int i = 0; i < MT; ++i) fa[i] = lds_rd16(lb + (aoff ^ (kk * 64)) + i * 2048);
  };
  auto mma = [&](bf16x8 (&fa)[MT], bf16x8 (&fw)[NT]) {
    __builtin_amdgcn_s_setprio(1);
#pragma unroll
    for (int i = 0; i < MT; ++i)
#pragma unroll
      for (int j = 0; j < NT; ++j) acc[i][j] = __builtin_amdgcn_mfma_f32_16x16x32_bf16(fw[j], fa[i], acc[i][j], 0, 0, 0);
    __builtin_amdgcn_s_setprio(0);
  };

  bf16x8 fa0[MT], fw0[NT], fa1[MT], fw1[NT];
  int buf = 0;
  WAIT_VM((NS - 2) * PER);
  __builtin_amdgcn_s_barrier();
  stage(NS - 1, NS - 1);
  ldfrag(fa0, fw0, 0, 0);
  ldfrag(fa1, fw1, 0, 1);
  mma(fa0, fw0);
#pragma unroll 1
  for (int kt = 1; kt < KT; ++kt) {
    const int pbuf = buf;
    buf = (buf + 1 == NS) ? 0 : buf + 1;
    if (kt + NS - 1 <= KT) {
      WAIT_VM((NS - 2) * PER);
    } else {
      WAIT_VM(0);
    }
    WAIT_LGKM0();
    __builtin_amdgcn_s_barrier();
    if (kt + NS - 1 < KT) stage(kt + NS - 1, pbuf);
    const u32 lb = buf * STAGE;
    ldfrag(fa0, fw0, lb, 0);
    mma(fa1, fw1);
    ldfrag(fa1, fw1, lb, 1);
    mma(fa0, fw0);
  }
  mma(fa1, fw1);

  if (EPI == 4) {
    static_assert(EPI != 4 || BM == 128, "yT tile");
    const __amdgpu_buffer_rsrc_t rY = mk_rsrc(a.ws + WS_YT, D * S * 2);
    WAIT_LGKM0();
    __builtin_amdgcn_s_barrier();
    constexpr int PY = BN * 256 / 1024 / NW;
#pragma unroll
    for (int i = 0; i < PY; ++i) {
      const int L = (i * NW + w) * 64 + lane, dr = L >> 4, c = (L & 15) ^ yswz(dr);
      glds16(rY, (i * NW + w) * 1024, dr * (S * 2) + c * 16, n0 * (S * 2) + m0 * 2);
    }
    WAIT_VM(0);
    __builtin_amdgcn_s_barrier();
  }
  const u16* xnp = (const u16*)(a.ws + WS_XN);
#pragma unroll
  for (int i = 0; i < MT; ++i) {
    float s1 = 0.f, s2 = 0.f;
#pragma unroll
    for (int j = 0; j < NT; j += 2) {
      const bool last = (j + 1 == NT);
      if (last && (i & 1)) continue;
      float v[8];
      f32x4 X = acc[i][j], Y = last ? acc[(i + 1) % MT][j] : acc[i][(j + 1) % NT];
      if (EPI == 3) {
        const int i2 = last ? (i + 1) % MT : i;
#pragma unroll
        for (int r = 0; r < 4; ++r) {
          const float t = X[r], u = Y[r];
          const float ea = __builtin_amdgcn_exp2f(t * __builtin_fmaf(t * t, rowc1[i], rowc0[i]));
          const float eb = __builtin_amdgcn_exp2f(u * __builtin_fmaf(u * u, rowc1[i2], rowc0[i2]));
          X[r] = t * __builtin_amdgcn_rcpf(__builtin_fmaf(ea, rowsd[i], rowsd[i]));
          Y[r] = u * __builtin_amdgcn_rcpf(__builtin_fmaf(eb, rowsd[i2], rowsd[i2]));
        }
      }
      if (EPI == 1) {
#pragma unroll
        for (int r = 0; r < 4; ++r) {
          X[r] = gelu_t(rbf(X[r]));
          Y[r] = gelu_t(rbf(Y[r]));
        }
      }
      const int row = m0 + wm * WTM + 16 * i + r16 + (last ? 16 * (g & 1) : 0);
      const int col = n0 + wn * WTN + 16 * j + (last ? 0 : 16 * (g & 1)) + 8 * (g >> 1);
      if (EPI == 1 || EPI == 3) {
        const u32x2 s0 = __builtin_amdgcn_permlane16_swap(pack2(X[0], X[1]), pack2(Y[0], Y[1]), false, false);
        const u32x2 s1_ = __builtin_amdgcn_permlane16_swap(pack2(X[2], X[3]), pack2(Y[2], Y[3]), false, false);
        const u32x4 o = {s0[0], s1_[0], s0[1], s1_[1]};
        if (EPI == 1) *(u32x4*)((u16*)(a.ws + WS_H) + (size_t)(row + 2) * D + col) = o;
        else *(u32x4*)((u16*)(a.ws + WS_G3) + (size_t)row * F + col) = o;
      } else {
        const u32x2 s0 = __builtin_amdgcn_permlane16_swap(pack2(X[0], X[1]), pack2(Y[0], Y[1]), false, false);
        const u32x2 s1_ = __builtin_amdgcn_permlane16_swap(pack2(X[2], X[3]), pack2(Y[2], Y[3]), false, false);
        const u32x4 hb = {s0[0], s1_[0], s0[1], s1_[1]};
        const u32x4 xn = *(const u32x4*)(xnp + (size_t)row * D + col);
        if (EPI == 2) {
          u32x4 o;
#pragma unroll
          for (int e = 0; e < 4; ++e) {
            o[e] = pack2(bf_lo(xn[e]) + bf_lo(hb[e]), bf_hi(xn[e]) + bf_hi(hb[e]));
            const float lo = bf_lo(o[e]), hi = bf_hi(o[e]);
            s1 += lo + hi;
            s2 += lo * lo + hi * hi;
          }
          *(u32x4*)((u16*)(a.ws + WS_X1) + (size_t)row * D + col) = o;
        } else {
          const u32x4 x1 = *(const u32x4*)((const u16*)(a.ws + WS_X1) + (size_t)row * D + col);
          const u32x4 xx = *(const u32x4*)(a.x + (size_t)row * D + col);
          const int dl = wn * WTN + 16 * j + 16 * (g & 1) + 8 * (g >> 1);
          const int q = r16 >> 2, p = lane & 3;
          const int tc = (wm * WTM + 16 * i) / 8 + (p >> 1);
          const int dr0 = dl + q, dr1 = dl + 4 + q;
          const s16x4 y0 = __builtin_amdgcn_ds_read_tr16_b64_v4i16(
              (lds_s16x4_t)(smem + dr0 * 256 + ((tc ^ yswz(dr0)) * 16) + 8 * (p & 1)));
          const s16x4 y1 = __builtin_amdgcn_ds_read_tr16_b64_v4i16(
              (lds_s16x4_t)(smem + dr1 * 256 + ((tc ^ yswz(dr1)) * 16) + 8 * (p & 1)));
          const u32x4 yy = __builtin_bit_cast(u32x4, __builtin_shufflevector(y0, y1, 0, 1, 2, 3, 4, 5, 6, 7));
          u32x4 o;
#pragma unroll
          for (int e = 0; e < 4; ++e) {
            float r2[2];
#pragma unroll
            for (int hh = 0; hh < 2; ++hh) {
              const float ob = rbf((hh ? bf_hi(x1[e]) : bf_lo(x1[e])) + (hh ? bf_hi(hb[e]) : bf_lo(hb[e])));
              const float gt = rbf(ob * __builtin_amdgcn_rcpf(1.0f + __builtin_amdgcn_exp2f(-1.442695041f * ob)));
              const float sv = rbf((hh ? bf_hi(xn[e]) : bf_lo(xn[e])) * (hh ? bf_hi(yy[e]) : bf_lo(yy[e])));
              r2[hh] = (hh ? bf_hi(xx[e]) : bf_lo(xx[e])) + rbf(sv * gt);
            }
            o[e] = pack2(r2[0], r2[1]);
          }
          *(u32x4*)(a.out + (size_t)row * D + col) = o;
        }
      }
      (void)v;
    }
    if (EPI == 2) {
      static_assert(EPI != 2 || (WGN == 1 && BN == 64), "stats slots");
      s1 += __shfl_xor(s1, 16, 64);
      s2 += __shfl_xor(s2, 16, 64);
      s1 += __shfl_xor(s1, 32, 64);
      s2 += __shfl_xor(s2, 32, 64);
      if (g == 0) {
        const int row = m0 + wm * WTM + 16 * i + r16;
        *(f32x2*)((float*)(a.ws + WS_ST) + (size_t)row * 32 + tn * 2) = f32x2{s1, s2};
      }
    }
  }
}

#define G1K gemm_kernel<1, 128, 64, 2, 2, 4, 1024, 1024, 1024, 4, 8>
#define G2K gemm_kernel<2, 128, 64, 4, 1, 4, 1024, 1024, 1024, 4, 8>
#define G3K gemm_kernel<3, 128, 224, 4, 2, 3, 1024, 1024, 3584, 8, 4>
#define G4K gemm_kernel<4, 128, 64, 2, 2, 4, 3584, 3584, 1024, 4, 8>
constexpr int G1_LDS = 4 * (128 + 64) * 128;
constexpr int G3_LDS = 3 * (128 + 224) * 128 + 2048;

extern "C" void kernel_launch(void* const* d_in, const int* in_sizes, int n_in, void* d_out, int out_size, void* d_ws,
                              size_t ws_size, hipStream_t stream) {
  const u16* xq = (const u16*)d_in[0];
  const u16* kk = (const u16*)d_in[1];
  const u16* vv = (const u16*)d_in[2];
  const u16* shk = (const u16*)d_in[3];
  const u16* ssm_k = (const u16*)d_in[4];
  const u16* ssm_D = (const u16*)d_in[5];
  const u16* w1 = (const u16*)d_in[7];
  const u16* w2 = (const u16*)d_in[8];
  const u16* w3 = (const u16*)d_in[9];
  const u16* w4 = (const u16*)d_in[10];
  char* ws = (char*)d_ws;
  (void)hipFuncSetAttribute((const void*)conv_kernel, hipFuncAttributeMaxDynamicSharedMemorySize, 4 * CV_CH);
  (void)hipFuncSetAttribute((const void*)G1K, hipFuncAttributeMaxDynamicSharedMemorySize, G1_LDS);
  (void)hipFuncSetAttribute((const void*)G2K, hipFuncAttributeMaxDynamicSharedMemorySize, G1_LDS);
  (void)hipFuncSetAttribute((const void*)G3K, hipFuncAttributeMaxDynamicSharedMemorySize, G3_LDS);
  (void)hipFuncSetAttribute((const void*)G4K, hipFuncAttributeMaxDynamicSharedMemorySize, G1_LDS);

  pre_kernel<<<PRE_KV_WG + PRE_LN_WG + PRE_CS_WG, 256, 16384, stream>>>(xq, kk, vv, shk, w3, ws);
  conv_kernel<<<256, 256, 4 * CV_CH, stream>>>(ssm_k, ssm_D, ws);
  GemmArgs g;
  g.x = xq;
  g.ws = ws;
  g.out = (u16*)d_out;
  g.A = (const u16*)(ws + WS_A1);
  g.W = w1;
  G1K<<<256, 256, G1_LDS, stream>>>(g);
  g.A = (const u16*)(ws + WS_H);
  g.W = w2;
  G2K<<<256, 256, G1_LDS, stream>>>(g);
  g.A = (const u16*)(ws + WS_X1);
  g.W = w3;
  G3K<<<256, 512, G3_LDS, stream>>>(g);
  g.A = (const u16*)(ws + WS_G3);
  g.W = w4;
  G4K<<<256, 256, G1_LDS, stream>>>(g);
}
```

```cpp
#include <hip/hip_runtime.h>
#include <hip/hip_bf16.h>
#include <stdint.h>

typedef unsigned short u16;
typedef unsigned int u32;
typedef __attribute__((ext_vector_type(8))) __bf16 bf16x8;
typedef __attribute__((ext_vector_type(4))) short s16x4;
typedef __attribute__((ext_vector_type(8))) short s16x8;
typedef __attribute__((ext_vector_type(4))) float f32x4;
typedef __attribute__((ext_vector_type(2))) float f32x2;
typedef __attribute__((ext_vector_type(2))) u32 u32x2;
typedef __attribute__((ext_vector_type(4))) u32 u32x4;
typedef __attribute__((ext_vector_type(2))) __bf16 bf16x2;
typedef __attribute__((address_space(3))) void* ldsptr_t;
typedef __attribute__((address_space(3))) s16x4* lds_s16x4_t;

constexpr int S = 2048, D = 1024, F = 3584;
constexpr int URS = 2304;

constexpr size_t WS_XN = 0;
constexpr size_t WS_A1 = WS_XN + (size_t)S * D * 2;
constexpr size_t WS_H = WS_A1 + (size_t)S * D * 2;
constexpr size_t WS_X1 = WS_H + (size_t)(S + 4) * D * 2;
constexpr size_t WS_G3 = WS_X1 + (size_t)S * D * 2;
constexpr size_t WS_UR = WS_G3 + (size_t)S * F * 2;
constexpr size_t WS_YT = WS_UR + (size_t)D * URS * 2 + 4096;
constexpr size_t WS_ST = WS_YT + (size_t)S * D * 2;
constexpr size_t WS_CS = WS_ST + (size_t)S * 16 * 8;
constexpr size_t WS_END = WS_CS + (size_t)16 * F * 4;

extern __shared__ __attribute__((aligned(16))) char smem[];

__device__ __forceinline__ float bf_lo(u32 v) { return __uint_as_float(v << 16); }
__device__ __forceinline__ float bf_hi(u32 v) { return __uint_as_float(v & 0xffff0000u); }
__device__ __forceinline__ u32 pack2(float lo, float hi) {
  bf16x2 r;
  r[0] = (__bf16)lo;
  r[1] = (__bf16)hi;
  return __builtin_bit_cast(u32, r);
}
__device__ __forceinline__ float rbf(float x) { return (float)(__bf16)x; }

__device__ __forceinline__ float wave_sum(float v) {
#pragma unroll
  for (int o = 32; o > 0; o >>= 1) v += __shfl_xor(v, o, 64);
  return v;
}

#define STAMP(i)
#define STAMP_DECL
#define STAMP_OUT(ws, kid)
#define WAIT_VM(n) asm volatile("s_waitcnt vmcnt(%0)" ::"n"(n) : "memory")
#define WAIT_LGKM0() asm volatile("s_waitcnt lgkmcnt(0)" ::: "memory")

__device__ __forceinline__ __amdgpu_buffer_rsrc_t mk_rsrc(const void* p, u32 bytes) {
  return __builtin_amdgcn_make_buffer_rsrc((void*)p, 0, bytes, 0x00020000);
}
__device__ __forceinline__ void glds16(__amdgpu_buffer_rsrc_t r, u32 lds_off, u32 voff, u32 soff) {
  asm volatile("s_mov_b32 m0, %0\n\ts_nop 0\n\tbuffer_load_dwordx4 %1, %2, %3 offen lds" ::"s"(lds_off), "v"(voff), "s"(r),
               "s"(soff)
               : "memory");
}

constexpr int PRE_KV_WG = 256, PRE_LN_WG = 512, PRE_CS_WG = 112;

__device__ __forceinline__ void ln_row16(const float (&x)[16], float (&y)[16]) {
  float s = 0.f;
#pragma unroll
  for (int i = 0; i < 16; ++i) s += x[i];
  const float mean = wave_sum(s) * (1.0f / D);
  float q = 0.f;
#pragma unroll
  for (int i = 0; i < 16; ++i) q += (x[i] - mean) * (x[i] - mean);
  const float var = wave_sum(q) * (1.0f / D);
  const float m = rbf(mean);
  const float sd = rbf(sqrtf(rbf(rbf(var) + 1e-5f)));
  const float rs = 1.0f / sd;
#pragma unroll
  for (int i = 0; i < 16; ++i) y[i] = rbf(rbf(x[i] - m) * rs);
}

__global__ __launch_bounds__(256) void pre_kernel(const u16* __restrict__ xq, const u16* __restrict__ kk,
                                                  const u16* __restrict__ vv, const u16* __restrict__ shk,
                                                  const u16* __restrict__ w3, char* __restrict__ ws) {
  const int tid = threadIdx.x;
  int b = blockIdx.x;
  if (b < PRE_KV_WG) {
    const int t0 = (b >> 4) * 128, d0 = (b & 15) * 64;
    const int tr = tid >> 3, dc = tid & 7;
    const int tb = t0 + 4 * tr, dd = d0 + 8 * dc;
    u32x4 kr[7], vr[4];
#pragma unroll
    for (int i = 0; i < 7; ++i) {
      const int t = tb - 3 + i;
      const u32x4 z = {0u, 0u, 0u, 0u};
      kr[i] = (t >= 0) ? *(const u32x4*)(kk + (size_t)(t < 0 ? 0 : t) * D + dd) : z;
    }
#pragma unroll
    for (int i = 0; i < 4; ++i) vr[i] = *(const u32x4*)(vv + (size_t)(tb + i) * D + dd);
    u32x4 sh[4];
#pragma unroll
    for (int i = 0; i < 4; ++i) sh[i] = *(const u32x4*)(shk + (size_t)dd * 4 + 8 * i);
    u32* T = (u32*)smem;
#pragma unroll
    for (int e = 0; e < 8; ++e) {
      const u32 s01 = sh[e >> 1][(e & 1) * 2], s23 = sh[e >> 1][(e & 1) * 2 + 1];
      const float c0 = bf_lo(s01), c1 = bf_hi(s01), c2 = bf_lo(s23), c3 = bf_hi(s23);
      float kf[7], o[4];
#pragma unroll
      for (int i = 0; i < 7; ++i) kf[i] = (e & 1) ? bf_hi(kr[i][e >> 1]) : bf_lo(kr[i][e >> 1]);
#pragma unroll
      for (int i = 0; i < 4; ++i) {
        float a = rbf(kf[i] * c3);
        a = rbf(a + rbf(kf[i + 1] * c2));
        a = rbf(a + rbf(kf[i + 2] * c1));
        a = rbf(a + rbf(kf[i + 3] * c0));
        const float vf = (e & 1) ? bf_hi(vr[i][e >> 1]) : bf_lo(vr[i][e >> 1]);
        o[i] = a * vf;
      }
      u32x2 pk = {pack2(o[3], o[2]), pack2(o[1], o[0])};
      const int dw = 62 - 2 * tr;
      const int dws = dw ^ (4 * dc);
      *(u32x2*)(T + (8 * dc + e) * 64 + dws) = pk;
    }
    __syncthreads();
    u16* ur = (u16*)(ws + WS_UR);
    const int x0 = S - 128 - t0;
#pragma unroll
    for (int i = 0; i < 4; ++i) {
      const int c = tid & 15, d = (tid >> 4) + 16 * i;
      const u32x4 val = *(const u32x4*)(T + d * 64 + ((4 * c) ^ (4 * (d >> 3))));
      *(u32x4*)(ur + (size_t)(d0 + d) * URS + x0 + 8 * c) = val;
    }
    if (t0 == 0) {
      const u32x4 z = {0u, 0u, 0u, 0u};
#pragma unroll
      for (int i = 0; i < 8; ++i) {
        const int c = tid & 31, d = (tid >> 5) + 8 * i;
        *(u32x4*)(ur + (size_t)(d0 + d) * URS + S + 8 * c) = z;
      }
    }
    if (b == 255) {
      const u32x4 z = {0u, 0u, 0u, 0u};
      u16* h = (u16*)(ws + WS_H);
      *(u32x4*)(h + tid * 8) = z;
      *(u32x4*)(h + (size_t)(S + 2) * D + tid * 8) = z;
    }
    return;
  }
  b -= PRE_KV_WG;
  if (b < PRE_LN_WG) {
    const int row = b * 4 + (tid >> 6), lane = tid & 63;
    const u32x4 r0 = *(const u32x4*)(xq + (size_t)row * D + lane * 8);
    const u32x4 r1 = *(const u32x4*)(xq + (size_t)row * D + 512 + lane * 8);
    float x[16], y[16], z[16];
#pragma unroll
    for (int i = 0; i < 4; ++i) {
      x[2 * i] = bf_lo(r0[i]);
      x[2 * i + 1] = bf_hi(r0[i]);
      x[8 + 2 * i] = bf_lo(r1[i]);
      x[8 + 2 * i + 1] = bf_hi(r1[i]);
    }
    ln_row16(x, y);
    ln_row16(y, z);
    u32x4 o0, o1, p0, p1;
#pragma unroll
    for (int i = 0; i < 4; ++i) {
      o0[i] = pack2(y[2 * i], y[2 * i + 1]);
      o1[i] = pack2(y[8 + 2 * i], y[8 + 2 * i + 1]);
      p0[i] = pack2(z[2 * i], z[2 * i + 1]);
      p1[i] = pack2(z[8 + 2 * i], z[8 + 2 * i + 1]);
    }
    u16* xn = (u16*)(ws + WS_XN);
    u16* a1 = (u16*)(ws + WS_A1);
    *(u32x4*)(xn + (size_t)row * D + lane * 8) = o0;
    *(u32x4*)(xn + (size_t)row * D + 512 + lane * 8) = o1;
    *(u32x4*)(a1 + (size_t)row * D + lane * 8) = p0;
    *(u32x4*)(a1 + (size_t)row * D + 512 + lane * 8) = p1;
    return;
  }
  b -= PRE_LN_WG;
  {
    const int rb = b / 7, cb = b % 7;
    const int w = tid >> 6, lane = tid & 63;
    const u16* p = w3 + (size_t)(rb * 64 + w * 16) * F + cb * 512 + lane * 8;
    u32x4 r[16];
#pragma unroll
    for (int i = 0; i < 16; ++i) r[i] = *(const u32x4*)(p + (size_t)i * F);
    float s[8];
#pragma unroll
    for (int e = 0; e < 8; ++e) s[e] = 0.f;
#pragma unroll
    for (int i = 0; i < 16; ++i)
#pragma unroll
      for (int e = 0; e < 4; ++e) {
        s[2 * e] += bf_lo(r[i][e]);
        s[2 * e + 1] += bf_hi(r[i][e]);
      }
    float* T = (float*)smem;
    *(f32x4*)(T + w * 512 + lane * 8) = f32x4{s[0], s[1], s[2], s[3]};
    *(f32x4*)(T + w * 512 + lane * 8 + 4) = f32x4{s[4], s[5], s[6], s[7]};
    __syncthreads();
    float* cs = (float*)(ws + WS_CS);
    const f32x2 a = *(const f32x2*)(T + 2 * tid), c = *(const f32x2*)(T + 512 + 2 * tid);
    const f32x2 e = *(const f32x2*)(T + 1024 + 2 * tid), g = *(const f32x2*)(T + 1536 + 2 * tid);
    *(f32x2*)(cs + (size_t)rb * F + cb * 512 + 2 * tid) = (a + c) + (e + g);
  }
}

constexpr int CV_U = 4640, CV_H = 4224, CV_CH = 4 * CV_U + 2 * CV_H;

__device__ __forceinline__ bf16x8 lds_rd16(u32 off) { return *(const bf16x8*)(smem + off); }

__global__ __launch_bounds__(256) void conv_kernel(const u16* __restrict__ ssm_k, const u16* __restrict__ ssm_D,
                                                   char* __restrict__ ws) {
  STAMP_DECL; STAMP(0);
  const int lane = threadIdx.x & 63;
  const int w = __builtin_amdgcn_readfirstlane(threadIdx.x >> 6);
  const int ch = blockIdx.x * 4 + w;
  const u32 base = w * CV_CH, hb = base + 4 * CV_U;
  const __amdgpu_buffer_rsrc_t rU = mk_rsrc(ws + WS_UR, D * URS * 2 + 4096);
  const __amdgpu_buffer_rsrc_t rH = mk_rsrc(ssm_k, D * S * 2);
  const u16* hrow = ssm_k + (size_t)ch * S;
  u32x4 hc[4];
  u32 hp[4];
#pragma unroll
  for (int i = 0; i < 4; ++i) {
    const int c = i * 64 + lane;
    hc[i] = *(const u32x4*)(hrow + 8 * c);
    hp[i] = c > 0 ? *(const u32*)(hrow + (c > 0 ? 8 * c - 2 : 0)) : 0u;
  }
  const float dcoef = bf_lo((u32)ssm_D[ch]);
#pragma unroll
  for (int c = 0; c < 4; ++c) {
#pragma unroll
    for (int i = 0; i < 4; ++i) glds16(rU, base + c * CV_U + i * 1024, lane * 16, ch * (URS * 2) + i * 1024 + (12 - 4 * c));
    if (lane < 32) glds16(rU, base + c * CV_U + 4096, lane * 16, ch * (URS * 2) + 4096 + (12 - 4 * c));
  }
#pragma unroll
  for (int i = 0; i < 4; ++i) glds16(rH, hb + CV_H + 112 + i * 1024, lane * 16, ch * (S * 2) + i * 1024);
  {
    const u32x4 z = {0u, 0u, 0u, 0u};
    if (lane < 7) *(u32x4*)(smem + hb + lane * 16) = z;
    else if (lane < 14) *(u32x4*)(smem + hb + CV_H + (lane - 7) * 16) = z;
  }
#pragma unroll
  for (int i = 0; i < 4; ++i) {
    u32x4 o;
    o[0] = __builtin_amdgcn_alignbit(hc[i][0], hp[i], 16);
    o[1] = __builtin_amdgcn_alignbit(hc[i][1], hc[i][0], 16);
    o[2] = __builtin_amdgcn_alignbit(hc[i][2], hc[i][1], 16);
    o[3] = __builtin_amdgcn_alignbit(hc[i][3], hc[i][2], 16);
    *(u32x4*)(smem + hb + 112 + (i * 64 + lane) * 16) = o;
  }
  WAIT_VM(0);
  WAIT_LGKM0();
  STAMP(1);
  const int r16 = lane & 15, g = lane >> 4;
  const u32 aaddr = hb + (r16 & 1) * CV_H + ((r16 >> 1) + g) * 16;
  const u32 baddr = base + (r16 & 3) * CV_U + (248 - 8 * (r16 >> 2) + g) * 16;
  f32x4 acc[8];
#pragma unroll
  for (int b = 0; b < 8; ++b) acc[b] = f32x4{0.f, 0.f, 0.f, 0.f};
#pragma unroll 1
  for (int r = 0; r < 8; ++r) {
    bf16x8 A[8], B[8];
    const u32 a0 = aaddr + 64 * r, b0 = baddr + 64 * r - 7 * 512;
#pragma unroll
    for (int p = 0; p < 8; ++p) {
      A[p] = lds_rd16(a0 + 512 * p);
      B[p] = lds_rd16(b0 + 512 * (7 - p));
    }
#pragma unroll
    for (int p = 0; p < 8; ++p)
#pragma unroll
      for (int q = 0; q + p < 8; ++q)
        acc[p + q] = __builtin_amdgcn_mfma_f32_16x16x32_bf16(A[p], B[q], acc[p + q], 0, 0, 0);
  }
  STAMP(2);
#pragma unroll
  for (int b = 0; b < 8; ++b) {
    const int t = 256 * b + 16 * g + 2 * (r16 & 3) + 64 * (r16 >> 2);
    *(u32*)(smem + hb + t * 2) = pack2(acc[b][0], acc[b][1]);
    *(u32*)(smem + hb + t * 2 + 16) = pack2(acc[b][2], acc[b][3]);
  }
  u16* yt = (u16*)(ws + WS_YT) + (size_t)ch * S;
#pragma unroll
  for (int i = 0; i < 4; ++i) {
    const int c = i * 64 + lane;
    const u32x4 y = *(const u32x4*)(smem + hb + c * 16);
    const u32x4 u = *(const u32x4*)(smem + base + 3 * CV_U + (255 - c) * 16);
    u32x4 o;
#pragma unroll
    for (int e = 0; e < 4; ++e) {
      const float lo = bf_lo(y[e]) + rbf(bf_hi(u[3 - e]) * dcoef);
      const float hi = bf_hi(y[e]) + rbf(bf_lo(u[3 - e]) * dcoef);
      o[e] = pack2(lo, hi);
    }
    *(u32x4*)(yt + c * 8) = o;
  }
  WAIT_VM(0);
  STAMP(3);
  STAMP_OUT(ws, 1);
}

struct GemmArgs {
  const u16* A;
  const u16* W;
  const u16* x;
  char* ws;
  u16* out;
};

template <int BN>
__device__ __forceinline__ int wswz(int r) {
  if (BN == 64) return 2 * (((r >> 1) & 1) + 2 * ((r >> 3) & 1));
  if (BN == 224) return 2 * ((r >> 3) & 1);
  return 2 * ((r & 3) + 4 * ((r >> 3) & 1));
}

__device__ __forceinline__ int yswz(int r) { return 2 * ((r & 3) + 4 * ((r >> 4) & 1)); }

__device__ __forceinline__ float gelu_t(float x) {
  const float c0 = -2.302208198f, c1 = -0.1029432397f;
  const float a = x * __builtin_fmaf(x * x, c1, c0);
  return x * __builtin_amdgcn_rcpf(1.0f + __builtin_amdgcn_exp2f(a));
}

template <int EPI, int BM, int BN, int WGM, int WGN, int NS, int K, int LDA, int LDW, int PM, int PN>
__global__ __launch_bounds__(512) void gemm_kernel(GemmArgs a) {
  constexpr int NW = 4;
  static_assert(WGM * WGN == 4, "4 consumers");
  constexpr int WTM = BM / WGM, WTN = BN / WGN, MT = WTM / 16, NT = WTN / 16;
  constexpr int WROW = BN * 2, CPR = BN / 8;
  constexpr int A_BYTES = BM * 128, W_BYTES = 64 * WROW, STAGE = A_BYTES + W_BYTES;
  constexpr int PA = (A_BYTES / 1024) / NW;
  constexpr int PW = (W_BYTES / 1024) / NW;
  static_assert((W_BYTES / 1024) % NW == 0, "pieces");
  constexpr int PER = PA + PW;
  constexpr int KT = K / 64;
  constexpr int TN = (EPI == 3 ? F : D) / BN;
  constexpr u32 XTRA = NS * STAGE;

  STAMP_DECL; STAMP(0);
  const int tid = threadIdx.x, lane = tid & 63;
  const int w8 = __builtin_amdgcn_readfirstlane(tid >> 6);
  const int w = w8 & 3;
  const int wm = w / WGN, wn = w % WGN;
  const int xcd = blockIdx.x & 7, idx = blockIdx.x >> 3;
  constexpr int XN_ = TN / PN;
  const int tm = (xcd / XN_) * PM + idx / PN, tn = (xcd % XN_) * PN + idx % PN;
  const int m0 = tm * BM, n0 = tn * BN;

  if (w8 >= 4) {
    const __amdgpu_buffer_rsrc_t rA = mk_rsrc(a.A, (EPI == 2 ? S + 4 : S) * LDA * 2);
    const __amdgpu_buffer_rsrc_t rW = mk_rsrc(a.W, K * LDW * 2);
    const u32 voffA = (lane >> 3) * (LDA * 2) + (((lane & 7) ^ (lane >> 3)) * 16);
    u32 voffW[PW];
#pragma unroll
    for (int i = 0; i < PW; ++i) {
      const int L = (i * NW + w) * 64 + lane;
      const int kr = L / CPR, c = (L % CPR) ^ wswz<BN>(kr);
      voffW[i] = kr * (LDW * 2) + c * 16;
    }
    auto stage = [&](int kt, int buf) {
      int arow = m0;
      if (EPI == 2) {
        const int grp = kt >> 2;
        arow += 2 + (grp == 0 ? -1 : grp == 1 ? 1 : grp == 2 ? -2 : 2);
      }
      const u32 sa = arow * (LDA * 2) + kt * 128;
      const u32 sw = kt * 64 * (LDW * 2) + n0 * 2;
      const u32 lb = buf * STAGE;
#pragma unroll
      for (int i = 0; i < PW; ++i) glds16(rW, lb + A_BYTES + (i * NW + w) * 1024, voffW[i], sw);
#pragma unroll
      for (int i = 0; i < PA; ++i) glds16(rA, lb + (i * NW + w) * 1024, voffA, sa + (i * NW + w) * 8 * (LDA * 2));
    };
#pragma unroll
    for (int s = 0; s < NS - 1; ++s) stage(s, s);
    if (EPI == 3) __builtin_amdgcn_s_barrier();
    int buf = NS - 1;
    WAIT_VM((NS - 2) * PER);
#pragma unroll 1
    for (int kt = 0; kt < KT - (NS - 1); ++kt) {
      __builtin_amdgcn_s_barrier();
      stage(kt + NS - 1, buf);
      buf = (buf + 1 == NS) ? 0 : buf + 1;
      WAIT_VM((NS - 2) * PER);
    }
    if (EPI == 4) {
      const __amdgpu_buffer_rsrc_t rY = mk_rsrc(a.ws + WS_YT, D * S * 2);
      constexpr int PY = BN * 256 / 1024 / NW;
#pragma unroll
      for (int i = 0; i < PY; ++i) {
        const int L = (i * NW + w) * 64 + lane, dr = L >> 4, c = (L & 15) ^ yswz(dr);
        glds16(rY, XTRA + (i * NW + w) * 1024, dr * (S * 2) + c * 16, n0 * (S * 2) + m0 * 2);
      }
    }
    constexpr int XP = (EPI == 4) ? BN * 256 / 1024 / NW : 0;
#pragma unroll
    for (int r = NS - 2; r >= 0; --r) {
      __builtin_amdgcn_s_barrier();
      if (r == 1) { WAIT_VM(0); }
      else if (r == 2) { WAIT_VM(1 * PER + XP); }
      else if (r == 3) { WAIT_VM(2 * PER + XP); }
      else if (r == 4) { WAIT_VM(3 * PER + XP); }
      else if (r == 5) { WAIT_VM(4 * PER + XP); }
    }
    return;
  }

  const int r16 = lane & 15, g = lane >> 4;
  const u32 aoff = (wm * WTM + r16) * 128 + ((g ^ (lane & 7)) * 16);
  u32 woff[NT];
  {
    const int q = r16 >> 2, p = lane & 3, kr = 8 * g + q;
#pragma unroll
    for (int j = 0; j < NT; ++j)
      woff[j] = A_BYTES + kr * WROW + (((wn * (WTN / 8) + 2 * j + (p >> 1)) ^ wswz<BN>(kr)) * 16) + 8 * (p & 1);
  }

  f32x4 acc[MT][NT];
  float rowc0[MT], rowc1[MT], rowsd[MT];
  if (EPI == 3) {
    float* tab = (float*)(smem + XTRA);
    const float* st = (const float*)(a.ws + WS_ST);
    const float* cs = (const float*)(a.ws + WS_CS);
    constexpr int TPR = 64 * NW / BM;
    {
      const int row = tid / TPR, part = tid % TPR;
      float s1 = 0.f, s2 = 0.f;
#pragma unroll
      for (int i = 0; i < 8 / TPR; ++i) {
        const f32x4 v = *(const f32x4*)(st + (size_t)(m0 + row) * 32 + (part * (8 / TPR) + i) * 4);
        s1 += v[0] + v[2];
        s2 += v[1] + v[3];
      }
#pragma unroll
      for (int o = 1; o < TPR; o <<= 1) {
        s1 += __shfl_xor(s1, o, 64);
        s2 += __shfl_xor(s2, o, 64);
      }
      const float mean = s1 * (1.0f / D);
      const float var = fmaxf(s2 * (1.0f / D) - mean * mean, 0.f);
      if (part == 0) {
        tab[2 * row] = rbf(mean);
        tab[2 * row + 1] = rbf(sqrtf(rbf(rbf(var) + 1e-5f)));
      }
    }
    if (tid < BN) {
      float c = 0.f;
#pragma unroll
      for (int i = 0; i < 16; ++i) c += cs[(size_t)i * F + n0 + tid];
      tab[2 * BM + tid] = c;
    }
    WAIT_LGKM0();
    __builtin_amdgcn_s_barrier();
#pragma unroll
    for (int i = 0; i < MT; ++i) {
      const f32x2 ms = *(const f32x2*)(tab + 2 * (wm * WTM + 16 * i + r16));
      const float rs = __builtin_amdgcn_rcpf(ms[1]);
      rowc0[i] = -2.302208198f * rs;
      rowc1[i] = -0.1029432397f * rs * rs * rs;
      rowsd[i] = ms[1];
#pragma unroll
      for (int j = 0; j < NT; ++j) {
        const f32x4 c = *(const f32x4*)(tab + 2 * BM + wn * WTN + 16 * j + 4 * g);
        acc[i][j] = c * (-ms[0]);
      }
    }
  } else {
#pragma unroll
    for (int i = 0; i < MT; ++i)
#pragma unroll
      for (int j = 0; j < NT; ++j) acc[i][j] = f32x4{0.f, 0.f, 0.f, 0.f};
  }

  auto ldfrag = [&](bf16x8 (&fa)[MT], bf16x8 (&fw)[NT], u32 lb, int kk) {
    fa[0] = lds_rd16(lb + (aoff ^ (kk * 64)));
#pragma unroll
    for (int j = 0; j < NT; ++j) {
      const s16x4 lo = __builtin_amdgcn_ds_read_tr16_b64_v4i16((lds_s16x4_t)(smem + lb + woff[j] + kk * 32 * WROW));
      const s16x4 hi = __builtin_amdgcn_ds_read_tr16_b64_v4i16((lds_s16x4_t)(smem + lb + woff[j] + (kk * 32 + 4) * WROW));
      fw[j] = __builtin_bit_cast(bf16x8, __builtin_shufflevector(lo, hi, 0, 1, 2, 3, 4, 5, 6, 7));
    }
#pragma unroll
    for (int i = 1; i < MT; ++i) fa[i] = lds_rd16(lb + (aoff ^ (kk * 64)) + i * 2048);
  };
  auto mma = [&](bf16x8 (&fa)[MT], bf16x8 (&fw)[NT]) {
#pragma unroll
    for (int i = 0; i < MT; ++i)
#pragma unroll
      for (int j = 0; j < NT; ++j) acc[i][j] = __builtin_amdgcn_mfma_f32_16x16x32_bf16(fw[j], fa[i], acc[i][j], 0, 0, 0);
  };
  auto weave = [&]() {
    constexpr int NR = MT + 2 * NT, NM = MT * NT;
#pragma unroll
    for (int i = 0; i < NM; ++i) {
      __builtin_amdgcn_sched_group_barrier(0x008, 1, 0);
      if (i < NR) __builtin_amdgcn_sched_group_barrier(0x100, 1, 0);
      if (i < NR) __builtin_amdgcn_sched_group_barrier(0x002, 1, 0);
    }
    __builtin_amdgcn_sched_barrier(0);
  };

  bf16x8 fa0[MT], fw0[NT], fa1[MT], fw1[NT];
  __builtin_amdgcn_s_barrier();
  STAMP(1);
  ldfrag(fa0, fw0, 0, 0);
  __builtin_amdgcn_sched_barrier(0);
  u32 lb = 0;
#pragma unroll 1
  for (int kt = 0; kt < KT - 1; ++kt) {
    ldfrag(fa1, fw1, lb, 1);
    mma(fa0, fw0);
    weave();
    lb = (lb + STAGE == NS * STAGE) ? 0 : lb + STAGE;
    WAIT_LGKM0();
    __builtin_amdgcn_s_barrier();
    __builtin_amdgcn_sched_barrier(0);
    ldfrag(fa0, fw0, lb, 0);
    mma(fa1, fw1);
    weave();
  }
  ldfrag(fa1, fw1, lb, 1);
  mma(fa0, fw0);
  weave();
  mma(fa1, fw1);
  STAMP(2);

  const u16* xnp = (const u16*)(a.ws + WS_XN);
#pragma unroll
  for (int i = 0; i < MT; ++i) {
    float s1 = 0.f, s2 = 0.f;
#pragma unroll
    for (int j = 0; j < NT; j += 2) {
      const bool last = (j + 1 == NT);
      if (last && (i & 1)) continue;
      float v[8];
      f32x4 X = acc[i][j], Y = last ? acc[(i + 1) % MT][j] : acc[i][(j + 1) % NT];
      if (EPI == 3) {
        const int i2 = last ? (i + 1) % MT : i;
#pragma unroll
        for (int r = 0; r < 4; ++r) {
          const float t = X[r], u = Y[r];
          const float ea = __builtin_amdgcn_exp2f(t * __builtin_fmaf(t * t, rowc1[i], rowc0[i]));
          const float eb = __builtin_amdgcn_exp2f(u * __builtin_fmaf(u * u, rowc1[i2], rowc0[i2]));
          X[r] = t * __builtin_amdgcn_rcpf(__builtin_fmaf(ea, rowsd[i], rowsd[i]));
          Y[r] = u * __builtin_amdgcn_rcpf(__builtin_fmaf(eb, rowsd[i2], rowsd[i2]));
        }
      }
      if (EPI == 1) {
#pragma unroll
        for (int r = 0; r < 4; ++r) {
          X[r] = gelu_t(rbf(X[r]));
          Y[r] = gelu_t(rbf(Y[r]));
        }
      }
      const int row = m0 + wm * WTM + 16 * i + r16 + (last ? 16 * (g & 1) : 0);
      const int col = n0 + wn * WTN + 16 * j + (last ? 0 : 16 * (g & 1)) + 8 * (g >> 1);
      if (EPI == 1 || EPI == 3) {
        const u32x2 s0 = __builtin_amdgcn_permlane16_swap(pack2(X[0], X[1]), pack2(Y[0], Y[1]), false, false);
        const u32x2 s1_ = __builtin_amdgcn_permlane16_swap(pack2(X[2], X[3]), pack2(Y[2], Y[3]), false, false);
        const u32x4 o = {s0[0], s1_[0], s0[1], s1_[1]};
        if (EPI == 1) *(u32x4*)((u16*)(a.ws + WS_H) + (size_t)(row + 2) * D + col) = o;
        else *(u32x4*)((u16*)(a.ws + WS_G3) + (size_t)row * F + col) = o;
      } else {
        const u32x2 s0 = __builtin_amdgcn_permlane16_swap(pack2(X[0], X[1]), pack2(Y[0], Y[1]), false, false);
        const u32x2 s1_ = __builtin_amdgcn_permlane16_swap(pack2(X[2], X[3]), pack2(Y[2], Y[3]), false, false);
        const u32x4 hb = {s0[0], s1_[0], s0[1], s1_[1]};
        const u32x4 xn = *(const u32x4*)(xnp + (size_t)row * D + col);
        if (EPI == 2) {
          u32x4 o;
#pragma unroll
          for (int e = 0; e < 4; ++e) {
            o[e] = pack2(bf_lo(xn[e]) + bf_lo(hb[e]), bf_hi(xn[e]) + bf_hi(hb[e]));
            const float lo = bf_lo(o[e]), hi = bf_hi(o[e]);
            s1 += lo + hi;
            s2 += lo * lo + hi * hi;
          }
          *(u32x4*)((u16*)(a.ws + WS_X1) + (size_t)row * D + col) = o;
        } else {
          const u32x4 x1 = *(const u32x4*)((const u16*)(a.ws + WS_X1) + (size_t)row * D + col);
          const u32x4 xx = *(const u32x4*)(a.x + (size_t)row * D + col);
          const int dl = wn * WTN + 16 * j + 16 * (g & 1) + 8 * (g >> 1);
          const int q = r16 >> 2, p = lane & 3;
          const int tc = (wm * WTM + 16 * i) / 8 + (p >> 1);
          const int dr0 = dl + q, dr1 = dl + 4 + q;
          const s16x4 y0 = __builtin_amdgcn_ds_read_tr16_b64_v4i16(
              (lds_s16x4_t)(smem + XTRA + dr0 * 256 + ((tc ^ yswz(dr0)) * 16) + 8 * (p & 1)));
          const s16x4 y1 = __builtin_amdgcn_ds_read_tr16_b64_v4i16(
              (lds_s16x4_t)(smem + XTRA + dr1 * 256 + ((tc ^ yswz(dr1)) * 16) + 8 * (p & 1)));
          const u32x4 yy = __builtin_bit_cast(u32x4, __builtin_shufflevector(y0, y1, 0, 1, 2, 3, 4, 5, 6, 7));
          u32x4 o;
#pragma unroll
          for (int e = 0; e < 4; ++e) {
            float r2[2];
#pragma unroll
            for (int hh = 0; hh < 2; ++hh) {
              const float ob = rbf((hh ? bf_hi(x1[e]) : bf_lo(x1[e])) + (hh ? bf_hi(hb[e]) : bf_lo(hb[e])));
              const float gt = rbf(ob * __builtin_amdgcn_rcpf(1.0f + __builtin_amdgcn_exp2f(-1.442695041f * ob)));
              const float sv = rbf((hh ? bf_hi(xn[e]) : bf_lo(xn[e])) * (hh ? bf_hi(yy[e]) : bf_lo(yy[e])));
              r2[hh] = (hh ? bf_hi(xx[e]) : bf_lo(xx[e])) + rbf(sv * gt);
            }
            o[e] = pack2(r2[0], r2[1]);
          }
          *(u32x4*)(a.out + (size_t)row * D + col) = o;
        }
      }
      (void)v;
    }
    if (EPI == 2) {
      static_assert(EPI != 2 || (WGN == 1 && BN == 64), "stats slots");
      s1 += __shfl_xor(s1, 16, 64);
      s2 += __shfl_xor(s2, 16, 64);
      s1 += __shfl_xor(s1, 32, 64);
      s2 += __shfl_xor(s2, 32, 64);
      if (g == 0) {
        const int row = m0 + wm * WTM + 16 * i + r16;
        *(f32x2*)((float*)(a.ws + WS_ST) + (size_t)row * 32 + tn * 2) = f32x2{s1, s2};
      }
    }
  }
  WAIT_VM(0);
  STAMP(3);
  STAMP_OUT(a.ws, EPI + 1);
}

#define G1K gemm_kernel<1, 128, 64, 2, 2, 6, 1024, 1024, 1024, 4, 8>
#define G2K gemm_kernel<2, 128, 64, 4, 1, 6, 1024, 1024, 1024, 4, 8>
#define G3K gemm_kernel<3, 128, 224, 2, 2, 3, 1024, 1024, 3584, 8, 4>
#define G4K gemm_kernel<4, 128, 64, 2, 2, 6, 3584, 3584, 1024, 4, 8>
constexpr int G1_LDS = 6 * (128 + 64) * 128 + 16384;
constexpr int G3_LDS = 3 * (128 + 224) * 128 + 2048;

extern "C" void kernel_launch(void* const* d_in, const int* in_sizes, int n_in, void* d_out, int out_size, void* d_ws,
                              size_t ws_size, hipStream_t stream) {
  const u16* xq = (const u16*)d_in[0];
  const u16* kk = (const u16*)d_in[1];
  const u16* vv = (const u16*)d_in[2];
  const u16* shk = (const u16*)d_in[3];
  const u16* ssm_k = (const u16*)d_in[4];
  const u16* ssm_D = (const u16*)d_in[5];
  const u16* w1 = (const u16*)d_in[7];
  const u16* w2 = (const u16*)d_in[8];
  const u16* w3 = (const u16*)d_in[9];
  const u16* w4 = (const u16*)d_in[10];
  char* ws = (char*)d_ws;
  (void)hipFuncSetAttribute((const void*)conv_kernel, hipFuncAttributeMaxDynamicSharedMemorySize, 4 * CV_CH);
  (void)hipFuncSetAttribute((const void*)G1K, hipFuncAttributeMaxDynamicSharedMemorySize, G1_LDS);
  (void)hipFuncSetAttribute((const void*)G2K, hipFuncAttributeMaxDynamicSharedMemorySize, G1_LDS);
  (void)hipFuncSetAttribute((const void*)G3K, hipFuncAttributeMaxDynamicSharedMemorySize, G3_LDS);
  (void)hipFuncSetAttribute((const void*)G4K, hipFuncAttributeMaxDynamicSharedMemorySize, G1_LDS);

  pre_kernel<<<PRE_KV_WG + PRE_LN_WG + PRE_CS_WG, 256, 16384, stream>>>(xq, kk, vv, shk, w3, ws);
  conv_kernel<<<256, 256, 4 * CV_CH, stream>>>(ssm_k, ssm_D, ws);
  GemmArgs g;
  g.x = xq;
  g.ws = ws;
  g.out = (u16*)d_out;
  g.A = (const u16*)(ws + WS_A1);
  g.W = w1;
  G1K<<<256, 512, G1_LDS, stream>>>(g);
  g.A = (const u16*)(ws + WS_H);
  g.W = w2;
  G2K<<<256, 512, G1_LDS, stream>>>(g);
  g.A = (const u16*)(ws + WS_X1);
  g.W = w3;
  G3K<<<256, 512, G3_LDS, stream>>>(g);
  g.A = (const u16*)(ws + WS_G3);
  g.W = w4;
  G4K<<<256, 512, G1_LDS, stream>>>(g);
}
```

```cpp
#include <hip/hip_runtime.h>
#include <hip/hip_bf16.h>
#include <stdint.h>
#define ROT 1
#define NS4 4

typedef unsigned short u16;
typedef unsigned int u32;
typedef __attribute__((ext_vector_type(8))) __bf16 bf16x8;
typedef __attribute__((ext_vector_type(4))) short s16x4;
typedef __attribute__((ext_vector_type(8))) short s16x8;
typedef __attribute__((ext_vector_type(4))) float f32x4;
typedef __attribute__((ext_vector_type(2))) float f32x2;
typedef __attribute__((ext_vector_type(2))) u32 u32x2;
typedef __attribute__((ext_vector_type(4))) u32 u32x4;
typedef __attribute__((ext_vector_type(2))) __bf16 bf16x2;
typedef __attribute__((address_space(3))) void* ldsptr_t;
typedef __attribute__((address_space(3))) s16x4* lds_s16x4_t;

constexpr int S = 2048, D = 1024, F = 3584;
constexpr int URS = 2304;

constexpr size_t WS_XN = 0;
constexpr size_t WS_A1 = WS_XN + (size_t)S * D * 2;
constexpr size_t WS_H = WS_A1 + (size_t)S * D * 2;
constexpr size_t WS_X1 = WS_H + (size_t)(S + 4) * D * 2;
constexpr size_t WS_G3 = WS_X1 + (size_t)S * D * 2;
constexpr size_t WS_UR = WS_G3 + (size_t)S * F * 2;
constexpr size_t WS_YT = WS_UR + (size_t)D * URS * 2 + 4096;
constexpr size_t WS_ST = WS_YT + (size_t)S * D * 2;
constexpr size_t WS_CS = WS_ST + (size_t)S * 16 * 8;
constexpr size_t WS_END = WS_CS + (size_t)16 * F * 4;

extern __shared__ __attribute__((aligned(16))) char smem[];

__device__ __forceinline__ float bf_lo(u32 v) { return __uint_as_float(v << 16); }
__device__ __forceinline__ float bf_hi(u32 v) { return __uint_as_float(v & 0xffff0000u); }
__device__ __forceinline__ u32 pack2(float lo, float hi) {
  bf16x2 r;
  r[0] = (__bf16)lo;
  r[1] = (__bf16)hi;
  return __builtin_bit_cast(u32, r);
}
__device__ __forceinline__ float rbf(float x) { return (float)(__bf16)x; }

__device__ __forceinline__ float wave_sum(float v) {
#pragma unroll
  for (int o = 32; o > 0; o >>= 1) v += __shfl_xor(v, o, 64);
  return v;
}

#define STAMP(i)
#define STAMP_DECL
#define STAMP_OUT(ws, kid)
#define WAIT_VM(n) asm volatile("s_waitcnt vmcnt(%0)" ::"n"(n) : "memory")
#define WAIT_LGKM0() asm volatile("s_waitcnt lgkmcnt(0)" ::: "memory")

__device__ __forceinline__ __amdgpu_buffer_rsrc_t mk_rsrc(const void* p, u32 bytes) {
  return __builtin_amdgcn_make_buffer_rsrc((void*)p, 0, bytes, 0x00020000);
}
#define DPOL ""
__device__ __forceinline__ void glds16(__amdgpu_buffer_rsrc_t r, u32 lds_off, u32 voff, u32 soff) {
  asm volatile("s_mov_b32 m0, %0\n\ts_nop 0\n\tbuffer_load_dwordx4 %1, %2, %3 offen" DPOL " lds" ::"s"(lds_off), "v"(voff), "s"(r),
               "s"(soff)
               : "memory");
}

template <int OFF>
__device__ __forceinline__ void stouch(const char* p) {
  asm volatile("s_load_dword s90, %0, %1" ::"s"(p), "n"(OFF) : "s90");
}
template <int N, int STRIDE, int I = 0>
__device__ __forceinline__ void stouch_n(const char* p) {
  if constexpr (I < N) {
    stouch<I * STRIDE>(p);
    stouch_n<N, STRIDE, I + 1>(p);
  }
}

constexpr int PRE_KV_WG = 256, PRE_LN_WG = 512, PRE_CS_WG = 112;

__device__ __forceinline__ void ln_row16(const float (&x)[16], float (&y)[16]) {
  float s = 0.f;
#pragma unroll
  for (int i = 0; i < 16; ++i) s += x[i];
  const float mean = wave_sum(s) * (1.0f / D);
  float q = 0.f;
#pragma unroll
  for (int i = 0; i < 16; ++i) q += (x[i] - mean) * (x[i] - mean);
  const float var = wave_sum(q) * (1.0f / D);
  const float m = rbf(mean);
  const float sd = rbf(sqrtf(rbf(rbf(var) + 1e-5f)));
  const float rs = 1.0f / sd;
#pragma unroll
  for (int i = 0; i < 16; ++i) y[i] = rbf(rbf(x[i] - m) * rs);
}

__global__ __launch_bounds__(256) void pre_kernel(const u16* __restrict__ xq, const u16* __restrict__ kk,
                                                  const u16* __restrict__ vv, const u16* __restrict__ shk,
                                                  const u16* __restrict__ w3, char* __restrict__ ws) {
  const int tid = threadIdx.x;
  int b = blockIdx.x;
  if (b < PRE_KV_WG) {
    const int t0 = (b >> 4) * 128, d0 = (b & 15) * 64;
    const int tr = tid >> 3, dc = tid & 7;
    const int tb = t0 + 4 * tr, dd = d0 + 8 * dc;
    u32x4 kr[7], vr[4];
#pragma unroll
    for (int i = 0; i < 7; ++i) {
      const int t = tb - 3 + i;
      const u32x4 z = {0u, 0u, 0u, 0u};
      kr[i] = (t >= 0) ? *(const u32x4*)(kk + (size_t)(t < 0 ? 0 : t) * D + dd) : z;
    }
#pragma unroll
    for (int i = 0; i < 4; ++i) vr[i] = *(const u32x4*)(vv + (size_t)(tb + i) * D + dd);
    u32x4 sh[4];
#pragma unroll
    for (int i = 0; i < 4; ++i) sh[i] = *(const u32x4*)(shk + (size_t)dd * 4 + 8 * i);
    u32* T = (u32*)smem;
#pragma unroll
    for (int e = 0; e < 8; ++e) {
      const u32 s01 = sh[e >> 1][(e & 1) * 2], s23 = sh[e >> 1][(e & 1) * 2 + 1];
      const float c0 = bf_lo(s01), c1 = bf_hi(s01), c2 = bf_lo(s23), c3 = bf_hi(s23);
      float kf[7], o[4];
#pragma unroll
      for (int i = 0; i < 7; ++i) kf[i] = (e & 1) ? bf_hi(kr[i][e >> 1]) : bf_lo(kr[i][e >> 1]);
#pragma unroll
      for (int i = 0; i < 4; ++i) {
        float a = rbf(kf[i] * c3);
        a = rbf(a + rbf(kf[i + 1] * c2));
        a = rbf(a + rbf(kf[i + 2] * c1));
        a = rbf(a + rbf(kf[i + 3] * c0));
        const float vf = (e & 1) ? bf_hi(vr[i][e >> 1]) : bf_lo(vr[i][e >> 1]);
        o[i] = a * vf;
      }
      u32x2 pk = {pack2(o[3], o[2]), pack2(o[1], o[0])};
      const int dw = 62 - 2 * tr;
      const int dws = dw ^ (4 * dc);
      *(u32x2*)(T + (8 * dc + e) * 64 + dws) = pk;
    }
    __syncthreads();
    u16* ur = (u16*)(ws + WS_UR);
    const int x0 = S - 128 - t0;
#pragma unroll
    for (int i = 0; i < 4; ++i) {
      const int c = tid & 15, d = (tid >> 4) + 16 * i;
      const u32x4 val = *(const u32x4*)(T + d * 64 + ((4 * c) ^ (4 * (d >> 3))));
      *(u32x4*)(ur + (size_t)(d0 + d) * URS + x0 + 8 * c) = val;
    }
    if (t0 == 0) {
      const u32x4 z = {0u, 0u, 0u, 0u};
#pragma unroll
      for (int i = 0; i < 8; ++i) {
        const int c = tid & 31, d = (tid >> 5) + 8 * i;
        *(u32x4*)(ur + (size_t)(d0 + d) * URS + S + 8 * c) = z;
      }
    }
    if (b == 255) {
      const u32x4 z = {0u, 0u, 0u, 0u};
      u16* h = (u16*)(ws + WS_H);
      *(u32x4*)(h + tid * 8) = z;
      *(u32x4*)(h + (size_t)(S + 2) * D + tid * 8) = z;
    }
    return;
  }
  b -= PRE_KV_WG;
  if (b < PRE_LN_WG) {
    const int row = b * 4 + (tid >> 6), lane = tid & 63;
    const u32x4 r0 = *(const u32x4*)(xq + (size_t)row * D + lane * 8);
    const u32x4 r1 = *(const u32x4*)(xq + (size_t)row * D + 512 + lane * 8);
    float x[16], y[16], z[16];
#pragma unroll
    for (int i = 0; i < 4; ++i) {
      x[2 * i] = bf_lo(r0[i]);
      x[2 * i + 1] = bf_hi(r0[i]);
      x[8 + 2 * i] = bf_lo(r1[i]);
      x[8 + 2 * i + 1] = bf_hi(r1[i]);
    }
    ln_row16(x, y);
    ln_row16(y, z);
    u32x4 o0, o1, p0, p1;
#pragma unroll
    for (int i = 0; i < 4; ++i) {
      o0[i] = pack2(y[2 * i], y[2 * i + 1]);
      o1[i] = pack2(y[8 + 2 * i], y[8 + 2 * i + 1]);
      p0[i] = pack2(z[2 * i], z[2 * i + 1]);
      p1[i] = pack2(z[8 + 2 * i], z[8 + 2 * i + 1]);
    }
    u16* xn = (u16*)(ws + WS_XN);
    u16* a1 = (u16*)(ws + WS_A1);
    *(u32x4*)(xn + (size_t)row * D + lane * 8) = o0;
    *(u32x4*)(xn + (size_t)row * D + 512 + lane * 8) = o1;
    *(u32x4*)(a1 + (size_t)row * D + lane * 8) = p0;
    *(u32x4*)(a1 + (size_t)row * D + 512 + lane * 8) = p1;
    return;
  }
  b -= PRE_LN_WG;
  {
    const int rb = b / 7, cb = b % 7;
    const int w = tid >> 6, lane = tid & 63;
    const u16* p = w3 + (size_t)(rb * 64 + w * 16) * F + cb * 512 + lane * 8;
    u32x4 r[16];
#pragma unroll
    for (int i = 0; i < 16; ++i) r[i] = *(const u32x4*)(p + (size_t)i * F);
    float s[8];
#pragma unroll
    for (int e = 0; e < 8; ++e) s[e] = 0.f;
#pragma unroll
    for (int i = 0; i < 16; ++i)
#pragma unroll
      for (int e = 0; e < 4; ++e) {
        s[2 * e] += bf_lo(r[i][e]);
        s[2 * e + 1] += bf_hi(r[i][e]);
      }
    float* T = (float*)smem;
    *(f32x4*)(T + w * 512 + lane * 8) = f32x4{s[0], s[1], s[2], s[3]};
    *(f32x4*)(T + w * 512 + lane * 8 + 4) = f32x4{s[4], s[5], s[6], s[7]};
    __syncthreads();
    float* cs = (float*)(ws + WS_CS);
    const f32x2 a = *(const f32x2*)(T + 2 * tid), c = *(const f32x2*)(T + 512 + 2 * tid);
    const f32x2 e = *(const f32x2*)(T + 1024 + 2 * tid), g = *(const f32x2*)(T + 1536 + 2 * tid);
    *(f32x2*)(cs + (size_t)rb * F + cb * 512 + 2 * tid) = (a + c) + (e + g);
  }
}

constexpr int CV_U = 4640, CV_H = 4224, CV_CH = 4 * CV_U + 2 * CV_H;

__device__ __forceinline__ bf16x8 lds_rd16(u32 off) { return *(const bf16x8*)(smem + off); }

__global__ __launch_bounds__(256) void conv_kernel(const u16* __restrict__ ssm_k, const u16* __restrict__ ssm_D,
                                                   char* __restrict__ ws) {
  STAMP_DECL; STAMP(0);
  const int lane = threadIdx.x & 63;
  const int w = __builtin_amdgcn_readfirstlane(threadIdx.x >> 6);
  const int ch = blockIdx.x * 4 + w;
  const u32 base = w * CV_CH, hb = base + 4 * CV_U;
  const __amdgpu_buffer_rsrc_t rU = mk_rsrc(ws + WS_UR, D * URS * 2 + 4096);
  const __amdgpu_buffer_rsrc_t rH = mk_rsrc(ssm_k, D * S * 2);
  const u16* hrow = ssm_k + (size_t)ch * S;
  u32x4 hc[4];
  u32 hp[4];
#pragma unroll
  for (int i = 0; i < 4; ++i) {
    const int c = i * 64 + lane;
    hc[i] = *(const u32x4*)(hrow + 8 * c);
    hp[i] = c > 0 ? *(const u32*)(hrow + (c > 0 ? 8 * c - 2 : 0)) : 0u;
  }
  const float dcoef = bf_lo((u32)ssm_D[ch]);
#pragma unroll
  for (int c = 0; c < 4; ++c) {
#pragma unroll
    for (int i = 0; i < 4; ++i) glds16(rU, base + c * CV_U + i * 1024, lane * 16, ch * (URS * 2) + i * 1024 + (12 - 4 * c));
    if (lane < 32) glds16(rU, base + c * CV_U + 4096, lane * 16, ch * (URS * 2) + 4096 + (12 - 4 * c));
  }
#pragma unroll
  for (int i = 0; i < 4; ++i) glds16(rH, hb + CV_H + 112 + i * 1024, lane * 16, ch * (S * 2) + i * 1024);
  {
    const u32x4 z = {0u, 0u, 0u, 0u};
    if (lane < 7) *(u32x4*)(smem + hb + lane * 16) = z;
    else if (lane < 14) *(u32x4*)(smem + hb + CV_H + (lane - 7) * 16) = z;
  }
#pragma unroll
  for (int i = 0; i < 4; ++i) {
    u32x4 o;
    o[0] = __builtin_amdgcn_alignbit(hc[i][0], hp[i], 16);
    o[1] = __builtin_amdgcn_alignbit(hc[i][1], hc[i][0], 16);
    o[2] = __builtin_amdgcn_alignbit(hc[i][2], hc[i][1], 16);
    o[3] = __builtin_amdgcn_alignbit(hc[i][3], hc[i][2], 16);
    *(u32x4*)(smem + hb + 112 + (i * 64 + lane) * 16) = o;
  }
  WAIT_VM(0);
  WAIT_LGKM0();
  STAMP(1);
  const int r16 = lane & 15, g = lane >> 4;
  const u32 aaddr = hb + (r16 & 1) * CV_H + ((r16 >> 1) + g) * 16;
  const u32 baddr = base + (r16 & 3) * CV_U + (248 - 8 * (r16 >> 2) + g) * 16;
  f32x4 acc[8];
#pragma unroll
  for (int b = 0; b < 8; ++b) acc[b] = f32x4{0.f, 0.f, 0.f, 0.f};
#pragma unroll 1
  for (int r = 0; r < 8; ++r) {
    bf16x8 A[8], B[8];
    const u32 a0 = aaddr + 64 * r, b0 = baddr + 64 * r - 7 * 512;
#pragma unroll
    for (int p = 0; p < 8; ++p) {
      A[p] = lds_rd16(a0 + 512 * p);
      B[p] = lds_rd16(b0 + 512 * (7 - p));
    }
#pragma unroll
    for (int p = 0; p < 8; ++p)
#pragma unroll
      for (int q = 0; q + p < 8; ++q)
        acc[p + q] = __builtin_amdgcn_mfma_f32_16x16x32_bf16(A[p], B[q], acc[p + q], 0, 0, 0);
  }
  STAMP(2);
#pragma unroll
  for (int b = 0; b < 8; ++b) {
    const int t = 256 * b + 16 * g + 2 * (r16 & 3) + 64 * (r16 >> 2);
    *(u32*)(smem + hb + t * 2) = pack2(acc[b][0], acc[b][1]);
    *(u32*)(smem + hb + t * 2 + 16) = pack2(acc[b][2], acc[b][3]);
  }
  u16* yt = (u16*)(ws + WS_YT) + (size_t)ch * S;
#pragma unroll
  for (int i = 0; i < 4; ++i) {
    const int c = i * 64 + lane;
    const u32x4 y = *(const u32x4*)(smem + hb + c * 16);
    const u32x4 u = *(const u32x4*)(smem + base + 3 * CV_U + (255 - c) * 16);
    u32x4 o;
#pragma unroll
    for (int e = 0; e < 4; ++e) {
      const float lo = bf_lo(y[e]) + rbf(bf_hi(u[3 - e]) * dcoef);
      const float hi = bf_hi(y[e]) + rbf(bf_lo(u[3 - e]) * dcoef);
      o[e] = pack2(lo, hi);
    }
    *(u32x4*)(yt + c * 8) = o;
  }
  WAIT_VM(0);
  STAMP(3);
  STAMP_OUT(ws, 1);
}

struct GemmArgs {
  const u16* A;
  const u16* W;
  const u16* x;
  char* ws;
  u16* out;
};

template <int BN>
__device__ __forceinline__ int wswz(int r) {
  if (BN == 64) return 2 * (((r >> 1) & 1) + 2 * ((r >> 3) & 1));
  if (BN == 224) return 2 * ((r >> 3) & 1);
  return 2 * ((r & 3) + 4 * ((r >> 3) & 1));
}

__device__ __forceinline__ int yswz(int r) { return 2 * ((r & 3) + 4 * ((r >> 4) & 1)); }

__device__ __forceinline__ float gelu_t(float x) {
  const float c0 = -2.302208198f, c1 = -0.1029432397f;
  const float a = x * __builtin_fmaf(x * x, c1, c0);
  return x * __builtin_amdgcn_rcpf(1.0f + __builtin_amdgcn_exp2f(a));
}

template <int EPI, int BM, int BN, int WGM, int WGN, int NS, int K, int LDA, int LDW, int PM, int PN>
__global__ __launch_bounds__(512) void gemm_kernel(GemmArgs a) {
  constexpr int NW = 4;
  static_assert(WGM * WGN == 4, "4 consumers");
  constexpr int WTM = BM / WGM, WTN = BN / WGN, MT = WTM / 16, NT = WTN / 16;
  constexpr int WROW = BN * 2, CPR = BN / 8;
  constexpr int A_BYTES = BM * 128, W_BYTES = 64 * WROW, STAGE = A_BYTES + W_BYTES;
  constexpr int PA = (A_BYTES / 1024) / NW;
  constexpr int PW = (W_BYTES / 1024) / NW;
  static_assert((W_BYTES / 1024) % NW == 0, "pieces");
  constexpr int PER = PA + PW;
  constexpr int KT = K / 64;
  constexpr int TN = (EPI == 3 ? F : D) / BN;
  constexpr u32 XTRA = NS * STAGE;

  STAMP_DECL; STAMP(0);
  const int tid = threadIdx.x, lane = tid & 63;
  const int w8 = __builtin_amdgcn_readfirstlane(tid >> 6);
  const int w = w8 & 3;
  const int wm = w / WGN, wn = w % WGN;
  const int xcd = blockIdx.x & 7, idx = blockIdx.x >> 3;
  constexpr int XN_ = TN / PN;
  const int tm = (xcd / XN_) * PM + idx / PN, tn = (xcd % XN_) * PN + idx % PN;
  const int m0 = tm * BM, n0 = tn * BN;

  if (w8 >= 4) {
    const __amdgpu_buffer_rsrc_t rA = mk_rsrc(a.A, (EPI == 2 ? S + 4 : S) * LDA * 2);
    const __amdgpu_buffer_rsrc_t rW = mk_rsrc(a.W, K * LDW * 2);
    const u32 voffA = (lane >> 3) * (LDA * 2) + (((lane & 7) ^ (lane >> 3)) * 16);
    u32 voffW[PW];
#pragma unroll
    for (int i = 0; i < PW; ++i) {
      const int L = (i * NW + w) * 64 + lane;
      const int kr = L / CPR, c = (L % CPR) ^ wswz<BN>(kr);
      voffW[i] = kr * (LDW * 2) + c * 16;
    }
    const int cls = (idx / PN + idx % PN) & 3;
    int nxt = (cls == 0) ? 1 : 0, cur = cls;
    auto stage = [&](int kt, int buf) {
      {
        const int u = kt / ROT, sub = kt % ROT;
        if (sub == 0 && u > 0) {
          if (((u - 1) & 3) == cls && u + 3 < KT / ROT) cur = u + 3;
          else {
            cur = nxt;
            nxt += ((nxt + 1) & 3) == cls ? 2 : 1;
          }
        }
        kt = cur * ROT + sub;
      }
      int arow = m0;
      if (EPI == 2) {
        const int grp = kt >> 2;
        arow += 2 + (grp == 0 ? -1 : grp == 1 ? 1 : grp == 2 ? -2 : 2);
      }
      const u32 sa = arow * (LDA * 2) + kt * 128;
      const u32 sw = kt * 64 * (LDW * 2) + n0 * 2;
      const u32 lb = buf * STAGE;
#pragma unroll
      for (int i = 0; i < PW; ++i) glds16(rW, lb + A_BYTES + (i * NW + w) * 1024, voffW[i], sw);
#pragma unroll
      for (int i = 0; i < PA; ++i) glds16(rA, lb + (i * NW + w) * 1024, voffA, sa + (i * NW + w) * 8 * (LDA * 2));
    };
#pragma unroll
    for (int s = 0; s < NS - 1; ++s) stage(s, s);
    if (EPI == 3) __builtin_amdgcn_s_barrier();
    int buf = NS - 1;
    WAIT_VM((NS - 2) * PER);
#pragma unroll 1
    for (int kt = 0; kt < KT - (NS - 1); ++kt) {
      __builtin_amdgcn_s_barrier();
      stage(kt + NS - 1, buf);
      buf = (buf + 1 == NS) ? 0 : buf + 1;
      WAIT_VM((NS - 2) * PER);
    }
    if (EPI == 4) {
      const __amdgpu_buffer_rsrc_t rY = mk_rsrc(a.ws + WS_YT, D * S * 2);
      constexpr int PY = BN * 256 / 1024 / NW;
#pragma unroll
      for (int i = 0; i < PY; ++i) {
        const int L = (i * NW + w) * 64 + lane, dr = L >> 4, c = (L & 15) ^ yswz(dr);
        glds16(rY, XTRA + (i * NW + w) * 1024, dr * (S * 2) + c * 16, n0 * (S * 2) + m0 * 2);
      }
    }
    constexpr int XP = (EPI == 4) ? BN * 256 / 1024 / NW : 0;
#pragma unroll
    for (int r = NS - 2; r >= 0; --r) {
      __builtin_amdgcn_s_barrier();
      if (r == 1) { WAIT_VM(0); }
      else if (r == 2) { WAIT_VM(1 * PER + XP); }
      else if (r == 3) { WAIT_VM(2 * PER + XP); }
      else if (r == 4) { WAIT_VM(3 * PER + XP); }
      else if (r == 5) { WAIT_VM(4 * PER + XP); }
    }
    WAIT_LGKM0();
    return;
  }

  const int r16 = lane & 15, g = lane >> 4;
  const u32 aoff = (wm * WTM + r16) * 128 + ((g ^ (lane & 7)) * 16);
  u32 woff[NT];
  {
    const int q = r16 >> 2, p = lane & 3, kr = 8 * g + q;
#pragma unroll
    for (int j = 0; j < NT; ++j)
      woff[j] = A_BYTES + kr * WROW + (((wn * (WTN / 8) + 2 * j + (p >> 1)) ^ wswz<BN>(kr)) * 16) + 8 * (p & 1);
  }

  f32x4 acc[MT][NT];
  float rowc0[MT], rowc1[MT], rowsd[MT];
  if (EPI == 3) {
    float* tab = (float*)(smem + XTRA);
    const float* st = (const float*)(a.ws + WS_ST);
    const float* cs = (const float*)(a.ws + WS_CS);
    constexpr int TPR = 64 * NW / BM;
    {
      const int row = tid / TPR, part = tid % TPR;
      float s1 = 0.f, s2 = 0.f;
#pragma unroll
      for (int i = 0; i < 8 / TPR; ++i) {
        const f32x4 v = *(const f32x4*)(st + (size_t)(m0 + row) * 32 + (part * (8 / TPR) + i) * 4);
        s1 += v[0] + v[2];
        s2 += v[1] + v[3];
      }
#pragma unroll
      for (int o = 1; o < TPR; o <<= 1) {
        s1 += __shfl_xor(s1, o, 64);
        s2 += __shfl_xor(s2, o, 64);
      }
      const float mean = s1 * (1.0f / D);
      const float var = fmaxf(s2 * (1.0f / D) - mean * mean, 0.f);
      if (part == 0) {
        tab[2 * row] = rbf(mean);
        tab[2 * row + 1] = rbf(sqrtf(rbf(rbf(var) + 1e-5f)));
      }
    }
    if (tid < BN) {
      float c = 0.f;
#pragma unroll
      for (int i = 0; i < 16; ++i) c += cs[(size_t)i * F + n0 + tid];
      tab[2 * BM + tid] = c;
    }
    WAIT_LGKM0();
    __builtin_amdgcn_s_barrier();
#pragma unroll
    for (int i = 0; i < MT; ++i) {
      const f32x2 ms = *(const f32x2*)(tab + 2 * (wm * WTM + 16 * i + r16));
      const float rs = __builtin_amdgcn_rcpf(ms[1]);
      rowc0[i] = -2.302208198f * rs;
      rowc1[i] = -0.1029432397f * rs * rs * rs;
      rowsd[i] = ms[1];
#pragma unroll
      for (int j = 0; j < NT; ++j) {
        const f32x4 c = *(const f32x4*)(tab + 2 * BM + wn * WTN + 16 * j + 4 * g);
        acc[i][j] = c * (-ms[0]);
      }
    }
  } else {
#pragma unroll
    for (int i = 0; i < MT; ++i)
#pragma unroll
      for (int j = 0; j < NT; ++j) acc[i][j] = f32x4{0.f, 0.f, 0.f, 0.f};
  }

  auto ldfrag = [&](bf16x8 (&fa)[MT], bf16x8 (&fw)[NT], u32 lb, int kk) {
    fa[0] = lds_rd16(lb + (aoff ^ (kk * 64)));
#pragma unroll
    for (int j = 0; j < NT; ++j) {
      const s16x4 lo = __builtin_amdgcn_ds_read_tr16_b64_v4i16((lds_s16x4_t)(smem + lb + woff[j] + kk * 32 * WROW));
      const s16x4 hi = __builtin_amdgcn_ds_read_tr16_b64_v4i16((lds_s16x4_t)(smem + lb + woff[j] + (kk * 32 + 4) * WROW));
      fw[j] = __builtin_bit_cast(bf16x8, __builtin_shufflevector(lo, hi, 0, 1, 2, 3, 4, 5, 6, 7));
    }
#pragma unroll
    for (int i = 1; i < MT; ++i) fa[i] = lds_rd16(lb + (aoff ^ (kk * 64)) + i * 2048);
  };
  auto mma = [&](bf16x8 (&fa)[MT], bf16x8 (&fw)[NT]) {
#pragma unroll
    for (int i = 0; i < MT; ++i)
#pragma unroll
      for (int j = 0; j < NT; ++j) acc[i][j] = __builtin_amdgcn_mfma_f32_16x16x32_bf16(fw[j], fa[i], acc[i][j], 0, 0, 0);
  };
  auto weave = [&]() {
    constexpr int NR = MT + 2 * NT, NM = MT * NT;
#pragma unroll
    for (int i = 0; i < NM; ++i) {
      __builtin_amdgcn_sched_group_barrier(0x008, 1, 0);
      if (i < NR) __builtin_amdgcn_sched_group_barrier(0x100, 1, 0);
      if (i < NR) __builtin_amdgcn_sched_group_barrier(0x002, 1, 0);
    }
    __builtin_amdgcn_sched_barrier(0);
  };

  bf16x8 fa0[MT], fw0[NT], fa1[MT], fw1[NT];
  __builtin_amdgcn_s_barrier();
  STAMP(1);
  ldfrag(fa0, fw0, 0, 0);
  __builtin_amdgcn_sched_barrier(0);
  u32 lb = 0;
#pragma unroll 1
  for (int kt = 0; kt < KT - 1; ++kt) {
    ldfrag(fa1, fw1, lb, 1);
    mma(fa0, fw0);
    weave();
    lb = (lb + STAGE == NS * STAGE) ? 0 : lb + STAGE;
    WAIT_LGKM0();
    __builtin_amdgcn_s_barrier();
    __builtin_amdgcn_sched_barrier(0);
    ldfrag(fa0, fw0, lb, 0);
    mma(fa1, fw1);
    weave();
  }
  ldfrag(fa1, fw1, lb, 1);
  mma(fa0, fw0);
  weave();
  mma(fa1, fw1);
  STAMP(2);

  const u16* xnp = (const u16*)(a.ws + WS_XN);
#pragma unroll
  for (int i = 0; i < MT; ++i) {
    float s1 = 0.f, s2 = 0.f;
#pragma unroll
    for (int j = 0; j < NT; j += 2) {
      const bool last = (j + 1 == NT);
      if (last && (i & 1)) continue;
      float v[8];
      f32x4 X = acc[i][j], Y = last ? acc[(i + 1) % MT][j] : acc[i][(j + 1) % NT];
      if (EPI == 3) {
        const int i2 = last ? (i + 1) % MT : i;
#pragma unroll
        for (int r = 0; r < 4; ++r) {
          const float t = X[r], u = Y[r];
          const float ea = __builtin_amdgcn_exp2f(t * __builtin_fmaf(t * t, rowc1[i], rowc0[i]));
          const float eb = __builtin_amdgcn_exp2f(u * __builtin_fmaf(u * u, rowc1[i2], rowc0[i2]));
          X[r] = t * __builtin_amdgcn_rcpf(__builtin_fmaf(ea, rowsd[i], rowsd[i]));
          Y[r] = u * __builtin_amdgcn_rcpf(__builtin_fmaf(eb, rowsd[i2], rowsd[i2]));
        }
      }
      if (EPI == 1) {
#pragma unroll
        for (int r = 0; r < 4; ++r) {
          X[r] = gelu_t(rbf(X[r]));
          Y[r] = gelu_t(rbf(Y[r]));
        }
      }
      const int row = m0 + wm * WTM + 16 * i + r16 + (last ? 16 * (g & 1) : 0);
      const int col = n0 + wn * WTN + 16 * j + (last ? 0 : 16 * (g & 1)) + 8 * (g >> 1);
      if (EPI == 1 || EPI == 3) {
        const u32x2 s0 = __builtin_amdgcn_permlane16_swap(pack2(X[0], X[1]), pack2(Y[0], Y[1]), false, false);
        const u32x2 s1_ = __builtin_amdgcn_permlane16_swap(pack2(X[2], X[3]), pack2(Y[2], Y[3]), false, false);
        const u32x4 o = {s0[0], s1_[0], s0[1], s1_[1]};
        if (EPI == 1) *(u32x4*)((u16*)(a.ws + WS_H) + (size_t)(row + 2) * D + col) = o;
        else *(u32x4*)((u16*)(a.ws + WS_G3) + (size_t)row * F + col) = o;
      } else {
        const u32x2 s0 = __builtin_amdgcn_permlane16_swap(pack2(X[0], X[1]), pack2(Y[0], Y[1]), false, false);
        const u32x2 s1_ = __builtin_amdgcn_permlane16_swap(pack2(X[2], X[3]), pack2(Y[2], Y[3]), false, false);
        const u32x4 hb = {s0[0], s1_[0], s0[1], s1_[1]};
        const u32x4 xn = *(const u32x4*)(xnp + (size_t)row * D + col);
        if (EPI == 2) {
          u32x4 o;
#pragma unroll
          for (int e = 0; e < 4; ++e) {
            o[e] = pack2(bf_lo(xn[e]) + bf_lo(hb[e]), bf_hi(xn[e]) + bf_hi(hb[e]));
            const float lo = bf_lo(o[e]), hi = bf_hi(o[e]);
            s1 += lo + hi;
            s2 += lo * lo + hi * hi;
          }
          *(u32x4*)((u16*)(a.ws + WS_X1) + (size_t)row * D + col) = o;
        } else {
          const u32x4 x1 = *(const u32x4*)((const u16*)(a.ws + WS_X1) + (size_t)row * D + col);
          const u32x4 xx = *(const u32x4*)(a.x + (size_t)row * D + col);
          const int dl = wn * WTN + 16 * j + 16 * (g & 1) + 8 * (g >> 1);
          const int q = r16 >> 2, p = lane & 3;
          const int tc = (wm * WTM + 16 * i) / 8 + (p >> 1);
          const int dr0 = dl + q, dr1 = dl + 4 + q;
          const s16x4 y0 = __builtin_amdgcn_ds_read_tr16_b64_v4i16(
              (lds_s16x4_t)(smem + XTRA + dr0 * 256 + ((tc ^ yswz(dr0)) * 16) + 8 * (p & 1)));
          const s16x4 y1 = __builtin_amdgcn_ds_read_tr16_b64_v4i16(
              (lds_s16x4_t)(smem + XTRA + dr1 * 256 + ((tc ^ yswz(dr1)) * 16) + 8 * (p & 1)));
          const u32x4 yy = __builtin_bit_cast(u32x4, __builtin_shufflevector(y0, y1, 0, 1, 2, 3, 4, 5, 6, 7));
          u32x4 o;
#pragma unroll
          for (int e = 0; e < 4; ++e) {
            float r2[2];
#pragma unroll
            for (int hh = 0; hh < 2; ++hh) {
              const float ob = rbf((hh ? bf_hi(x1[e]) : bf_lo(x1[e])) + (hh ? bf_hi(hb[e]) : bf_lo(hb[e])));
              const float gt = rbf(ob * __builtin_amdgcn_rcpf(1.0f + __builtin_amdgcn_exp2f(-1.442695041f * ob)));
              const float sv = rbf((hh ? bf_hi(xn[e]) : bf_lo(xn[e])) * (hh ? bf_hi(yy[e]) : bf_lo(yy[e])));
              r2[hh] = (hh ? bf_hi(xx[e]) : bf_lo(xx[e])) + rbf(sv * gt);
            }
            o[e] = pack2(r2[0], r2[1]);
          }
          *(u32x4*)(a.out + (size_t)row * D + col) = o;
        }
      }
      (void)v;
    }
    if (EPI == 2) {
      static_assert(EPI != 2 || (WGN == 1 && BN == 64), "stats slots");
      s1 += __shfl_xor(s1, 16, 64);
      s2 += __shfl_xor(s2, 16, 64);
      s1 += __shfl_xor(s1, 32, 64);
      s2 += __shfl_xor(s2, 32, 64);
      if (g == 0) {
        const int row = m0 + wm * WTM + 16 * i + r16;
        *(f32x2*)((float*)(a.ws + WS_ST) + (size_t)row * 32 + tn * 2) = f32x2{s1, s2};
      }
    }
  }
  WAIT_VM(0);
  STAMP(3);
  STAMP_OUT(a.ws, EPI + 1);
}

#define G1K gemm_kernel<1, 128, 64, 2, 2, NS4, 1024, 1024, 1024, 4, 8>
#define G2K gemm_kernel<2, 128, 64, 4, 1, NS4, 1024, 1024, 1024, 4, 8>
#define G3K gemm_kernel<3, 128, 224, 2, 2, 3, 1024, 1024, 3584, 8, 4>
#define G4K gemm_kernel<4, 128, 64, 2, 2, NS4, 3584, 3584, 1024, 4, 8>
constexpr int G1_LDS = 6 * (128 + 64) * 128 + 16384;
constexpr int G3_LDS = 3 * (128 + 224) * 128 + 2048;

extern "C" void kernel_launch(void* const* d_in, const int* in_sizes, int n_in, void* d_out, int out_size, void* d_ws,
                              size_t ws_size, hipStream_t stream) {
  const u16* xq = (const u16*)d_in[0];
  const u16* kk = (const u16*)d_in[1];
  const u16* vv = (const u16*)d_in[2];
  const u16* shk = (const u16*)d_in[3];
  const u16* ssm_k = (const u16*)d_in[4];
  const u16* ssm_D = (const u16*)d_in[5];
  const u16* w1 = (const u16*)d_in[7];
  const u16* w2 = (const u16*)d_in[8];
  const u16* w3 = (const u16*)d_in[9];
  const u16* w4 = (const u16*)d_in[10];
  char* ws = (char*)d_ws;
  (void)hipFuncSetAttribute((const void*)conv_kernel, hipFuncAttributeMaxDynamicSharedMemorySize, 4 * CV_CH);
  (void)hipFuncSetAttribute((const void*)G1K, hipFuncAttributeMaxDynamicSharedMemorySize, G1_LDS);
  (void)hipFuncSetAttribute((const void*)G2K, hipFuncAttributeMaxDynamicSharedMemorySize, G1_LDS);
  (void)hipFuncSetAttribute((const void*)G3K, hipFuncAttributeMaxDynamicSharedMemorySize, G3_LDS);
  (void)hipFuncSetAttribute((const void*)G4K, hipFuncAttributeMaxDynamicSharedMemorySize, G1_LDS);

  pre_kernel<<<PRE_KV_WG + PRE_LN_WG + PRE_CS_WG, 256, 16384, stream>>>(xq, kk, vv, shk, w3, ws);
  conv_kernel<<<256, 256, 4 * CV_CH, stream>>>(ssm_k, ssm_D, ws);
  GemmArgs g;
  g.x = xq;
  g.ws = ws;
  g.out = (u16*)d_out;
  g.A = (const u16*)(ws + WS_A1);
  g.W = w1;
  G1K<<<256, 512, G1_LDS, stream>>>(g);
  g.A = (const u16*)(ws + WS_H);
  g.W = w2;
  G2K<<<256, 512, G1_LDS, stream>>>(g);
  g.A = (const u16*)(ws + WS_X1);
  g.W = w3;
  G3K<<<256, 512, G3_LDS, stream>>>(g);
  g.A = (const u16*)(ws + WS_G3);
  g.W = w4;
  G4K<<<256, 512, G1_LDS, stream>>>(g);
}
```

```cpp
#include <hip/hip_runtime.h>
#include <hip/hip_bf16.h>
#include <stdint.h>
#include <type_traits>

typedef unsigned short u16;
typedef unsigned int u32;
typedef __attribute__((ext_vector_type(8))) __bf16 bf16x8;
typedef __attribute__((ext_vector_type(4))) short s16x4;
typedef __attribute__((ext_vector_type(4))) float f32x4;
typedef __attribute__((ext_vector_type(2))) float f32x2;
typedef __attribute__((ext_vector_type(2))) u32 u32x2;
typedef __attribute__((ext_vector_type(4))) u32 u32x4;
typedef __attribute__((ext_vector_type(2))) __bf16 bf16x2;
typedef __attribute__((address_space(3))) s16x4* lds_s16x4_t;
typedef __attribute__((address_space(3))) volatile u32x2* lds_vu32x2_t;

constexpr int S = 2048, D = 1024, F = 3584;

constexpr size_t WS_H = 0;
constexpr size_t WS_X1 = WS_H + (size_t)(S + 4) * D * 2;
constexpr size_t WS_G3 = WS_X1 + (size_t)S * D * 2;
constexpr size_t WS_UR = WS_G3 + (size_t)S * F * 2;
constexpr size_t WS_YT = WS_UR + (size_t)D * S * 2 + 4096;
constexpr size_t WS_ST = WS_YT + (size_t)S * D * 2;
constexpr size_t WS_CS = WS_ST + (size_t)S * 16 * 8;
constexpr size_t WS_XS = WS_CS + (size_t)4 * F * 4;

extern __shared__ __attribute__((aligned(16))) char smem[];

__device__ __forceinline__ float bf_lo(u32 v) { return __uint_as_float(v << 16); }
__device__ __forceinline__ float bf_hi(u32 v) { return __uint_as_float(v & 0xffff0000u); }
__device__ __forceinline__ f32x2 bf_2(u32 v) { return f32x2{bf_lo(v), bf_hi(v)}; }
__device__ __forceinline__ u32 pack2(float lo, float hi) {
  bf16x2 r;
  r[0] = (__bf16)lo;
  r[1] = (__bf16)hi;
  return __builtin_bit_cast(u32, r);
}
__device__ __forceinline__ float rbf(float x) { return (float)(__bf16)x; }
__device__ __forceinline__ f32x2 fma2(f32x2 a, f32x2 b, f32x2 c) { return __builtin_elementwise_fma(a, b, c); }
__device__ __forceinline__ f32x2 dup2(float a) { return f32x2{a, a}; }

#define STAMP(i)
#define STAMP_DECL
#define STAMP_OUT(ws, kid)
#define WAIT_VM(n) asm volatile("s_waitcnt vmcnt(%0)" ::"n"(n) : "memory")
#define WAIT_LGKM0() __builtin_amdgcn_s_waitcnt(0xC07F)
#define PIN() __builtin_amdgcn_sched_barrier(0)

__device__ __forceinline__ __amdgpu_buffer_rsrc_t mk_rsrc(const void* p, u32 bytes) {
  return __builtin_amdgcn_make_buffer_rsrc((void*)p, 0, bytes, 0x00020000);
}
__device__ __forceinline__ void glds16(__amdgpu_buffer_rsrc_t r, u32 lds_off, u32 voff, u32 soff) {
  asm volatile("s_mov_b32 m0, %0\n\ts_nop 0\n\tbuffer_load_dwordx4 %1, %2, %3 offen lds" ::"s"(lds_off), "v"(voff), "s"(r),
               "s"(soff)
               : "memory");
}
__device__ __forceinline__ void touch(__amdgpu_buffer_rsrc_t r, u32 lds_off, u32 voff, u32 soff) {
  asm volatile("s_mov_b32 m0, %0\n\ts_nop 0\n\tbuffer_load_dword %1, %2, %3 offen lds" ::"s"(lds_off), "v"(voff), "s"(r),
               "s"(soff)
               : "memory");
}
__device__ __forceinline__ bf16x8 lds_rd16(u32 off) { return *(const bf16x8*)(smem + off); }
__device__ __forceinline__ s16x4 lds_tr(u32 off) { return __builtin_amdgcn_ds_read_tr16_b64_v4i16((lds_s16x4_t)(smem + off)); }

struct Args {
  const u16 *x, *k, *v, *shk, *ssm_k, *ssm_D, *w1, *w2, *w3, *w4;
  char* ws;
  u16* out;
};

__device__ __forceinline__ void side1(const Args& a, int xcd, int j16) {
  STAMP_DECL; STAMP(0);
  const int tid = threadIdx.x, lane = tid & 63;
  const int w = __builtin_amdgcn_readfirstlane(tid >> 6);
  const u32 dummy = 40960 + w * 256;
  {
    const int q = j16 * 8 + w, sub = q & 31;
    if (sub < 16) {
      const __amdgpu_buffer_rsrc_t r = mk_rsrc(a.x, S * D * 2);
#pragma unroll
      for (int i = 1; i < 4; ++i)
        touch(r, dummy, (lane >> 1) * 2048 + (lane & 1) * 64, ((xcd >> 1) * 512 + sub * 32) * 2048 + (4 * i + (q >> 5)) * 128);
    } else {
      const __amdgpu_buffer_rsrc_t r = mk_rsrc(a.w1, D * D * 2);
#pragma unroll
      for (int i = 1; i < 4; ++i)
        touch(r, dummy, (lane >> 4) * 2048 + (lane & 15) * 64, ((4 * i + (q >> 5)) * 64 + (sub - 16) * 4) * 2048 + (xcd & 1) * 1024);
    }
  }
  PIN();
  const int t0 = j16 * 128, d0 = xcd * 128;
  const int tr = tid >> 4, dc = tid & 15;
  const int tb = t0 + 4 * tr, dd = d0 + 8 * dc;
  u32x4 kr[7], vr[4], sh[4];
#pragma unroll
  for (int i = 0; i < 4; ++i) sh[i] = *(const u32x4*)(a.shk + (size_t)dd * 4 + 8 * i);
#pragma unroll
  for (int i = 0; i < 7; ++i) {
    const int t = tb - 3 + i;
    kr[i] = *(const u32x4*)(a.k + (size_t)(t < 0 ? 0 : t) * D + dd);
  }
#pragma unroll
  for (int i = 0; i < 4; ++i) vr[i] = __builtin_nontemporal_load((const u32x4*)(a.v + (size_t)(tb + i) * D + dd));
  const int u = xcd * 16 + j16;
  const int cb = u < 112 ? u % 28 : 0, rb = u < 112 ? u / 28 : 0;
  u32x4 wr[8];
  if (u < 112) {
#pragma unroll
    for (int i = 0; i < 8; ++i) wr[i] = *(const u32x4*)(a.w3 + (size_t)(rb * 256 + tr + 32 * i) * F + cb * 128 + 8 * dc);
  }
  PIN();
  if (tb == 0) {
#pragma unroll
    for (int i = 0; i < 3; ++i) kr[i] = u32x4{0u, 0u, 0u, 0u};
  }
  u32* T = (u32*)smem;
#pragma unroll
  for (int e = 0; e < 8; ++e) {
    const u32 s01 = sh[e >> 1][(e & 1) * 2], s23 = sh[e >> 1][(e & 1) * 2 + 1];
    const float c0 = bf_lo(s01), c1 = bf_hi(s01), c2 = bf_lo(s23), c3 = bf_hi(s23);
    float kf[7], o[4];
#pragma unroll
    for (int i = 0; i < 7; ++i) kf[i] = (e & 1) ? bf_hi(kr[i][e >> 1]) : bf_lo(kr[i][e >> 1]);
#pragma unroll
    for (int i = 0; i < 4; ++i) {
      const float s = __builtin_fmaf(kf[i + 3], c0, __builtin_fmaf(kf[i + 2], c1, __builtin_fmaf(kf[i + 1], c2, kf[i] * c3)));
      o[i] = s * ((e & 1) ? bf_hi(vr[i][e >> 1]) : bf_lo(vr[i][e >> 1]));
    }
    *(u32x2*)(T + (8 * dc + e) * 64 + ((62 - 2 * tr) ^ (4 * dc))) = u32x2{pack2(o[3], o[2]), pack2(o[1], o[0])};
  }
  WAIT_LGKM0();
  __builtin_amdgcn_s_barrier();
  u16* ur = (u16*)(a.ws + WS_UR);
#pragma unroll
  for (int i = 0; i < 4; ++i) {
    const int d = tr + 32 * i;
    *(u32x4*)(ur + (size_t)(d0 + d) * S + (S - 128 - t0) + 8 * dc) = *(const u32x4*)(T + d * 64 + ((4 * dc) ^ (4 * (d >> 3))));
  }
  STAMP(1);
  {
    const __amdgpu_buffer_rsrc_t r = mk_rsrc(a.w2, D * D * 2);
    const int q = j16 * 8 + w;
#pragma unroll
    for (int i = 0; i < 2; ++i) touch(r, dummy, (lane >> 4) * 2048 + (lane & 15) * 64, (i * 512 + q * 4) * 2048 + (xcd & 1) * 1024);
    touch(mk_rsrc(a.ssm_k, D * S * 2), dummy, lane * 64, (u * 8 + w) * 4096);
  }
  if (u < 112) {
    f32x2 s[4];
#pragma unroll
    for (int e = 0; e < 4; ++e) {
      s[e] = bf_2(wr[0][e]);
#pragma unroll
      for (int i = 1; i < 8; ++i) s[e] += bf_2(wr[i][e]);
#pragma unroll
      for (int h = 0; h < 2; ++h) {
        s[e][h] += __shfl_xor(s[e][h], 16, 64);
        s[e][h] += __shfl_xor(s[e][h], 32, 64);
      }
    }
    float* C = (float*)(smem + 32768);
    if (lane < 16) {
      *(f32x4*)(C + w * 128 + 8 * dc) = f32x4{s[0][0], s[0][1], s[1][0], s[1][1]};
      *(f32x4*)(C + w * 128 + 8 * dc + 4) = f32x4{s[2][0], s[2][1], s[3][0], s[3][1]};
    }
    WAIT_LGKM0();
    __builtin_amdgcn_s_barrier();
    if (tid < 128) {
      float c = 0.f;
#pragma unroll
      for (int i = 0; i < 8; ++i) c += C[i * 128 + tid];
      ((float*)(a.ws + WS_CS))[rb * F + cb * 128 + tid] = c;
    }
  } else if (u == 127) {
    const int kt = tid >> 5, r = (tid >> 3) & 3;
    *(u32x4*)(a.ws + WS_H + ((size_t)kt * (S + 4) + (r < 2 ? r : S + r)) * 128 + (tid & 7) * 16) = u32x4{0u, 0u, 0u, 0u};
  }
  STAMP(2);
  WAIT_VM(0);
  STAMP(3);
  STAMP_OUT(a.ws, 0);
}

constexpr int CV_H = 4576, CV_U = 4192, CV_CH = 2 * CV_H + 2 * CV_U;

__device__ __forceinline__ void conv(const Args& a, int u) {
  STAMP_DECL; STAMP(0);
  const int lane = threadIdx.x & 63;
  const int w = __builtin_amdgcn_readfirstlane(threadIdx.x >> 6);
  const int ch = u * 8 + w;
  const u32 hb = w * CV_CH, ub = hb + 2 * CV_H;
  const __amdgpu_buffer_rsrc_t rU = mk_rsrc(a.ws + WS_UR, D * S * 2 + 4096);
  const __amdgpu_buffer_rsrc_t rH = mk_rsrc(a.ssm_k, D * S * 2);
  const u16* hrow = a.ssm_k + (size_t)ch * S;
#define CV_SLEEP 8
  if (w >= 4) __builtin_amdgcn_s_sleep(CV_SLEEP);
  u32x4 hc[4];
  u32 hp[4];
#pragma unroll
  for (int i = 0; i < 4; ++i) {
    const int c = i * 64 + lane;
    hc[i] = *(const u32x4*)(hrow + 8 * c);
    hp[i] = *(const u32*)(hrow + (c > 0 ? 8 * c - 2 : 0));
  }
  const float dcoef = bf_lo((u32)a.ssm_D[ch]);
  PIN();
#pragma unroll
  for (int i = 0; i < 4; ++i) glds16(rH, hb + CV_H + 448 + i * 1024, lane * 16, ch * (S * 2) + i * 1024);
#pragma unroll
  for (int i = 0; i < 4; ++i) {
    glds16(rU, ub + CV_U + i * 1024, lane * 16, ch * (S * 2) + i * 1024);
    glds16(rU, ub + i * 1024, lane * 16, ch * (S * 2) + i * 1024 + 4);
  }
  {
    const u32x4 z = {0u, 0u, 0u, 0u};
    const u32 zo = lane < 28 ? hb + lane * 16 : lane < 56 ? hb + CV_H + (lane - 28) * 16 : ub + ((lane >> 2) & 1) * CV_U + 4096 + (lane & 3) * 16;
    *(u32x4*)(smem + zo) = z;
  }
  if (lane == 0) hp[0] = 0u;
#pragma unroll
  for (int i = 0; i < 4; ++i) {
    u32x4 o;
    o[0] = __builtin_amdgcn_alignbit(hc[i][0], hp[i], 16);
    o[1] = __builtin_amdgcn_alignbit(hc[i][1], hc[i][0], 16);
    o[2] = __builtin_amdgcn_alignbit(hc[i][2], hc[i][1], 16);
    o[3] = __builtin_amdgcn_alignbit(hc[i][3], hc[i][2], 16);
    *(u32x4*)(smem + hb + 448 + (i * 64 + lane) * 16) = o;
  }
  WAIT_VM(0);
  if (lane == 0) *(u32*)(smem + ub + 4092) = 0u;
  {
    const __amdgpu_buffer_rsrc_t r = mk_rsrc(a.w4, F * D * 2);
    touch(r, 8 * CV_CH + w * 256, lane * 64, ch * 7168);
    if (lane < 48) touch(r, 8 * CV_CH + w * 256, lane * 64, ch * 7168 + 4096);
  }
  STAMP(1);
  const int r16 = lane & 15, g = lane >> 4;
  const u32 aaddr = hb + (r16 & 1) * CV_H + 64 * (r16 >> 1) + 16 * g;
  const u32 baddr = ub + (r16 & 1) * CV_U + 56 - 8 * (r16 >> 1) + 16 * g;
  f32x4 acc[8];
#pragma unroll
  for (int b = 0; b < 8; ++b) acc[b] = f32x4{0.f, 0.f, 0.f, 0.f};
#pragma unroll 1
  for (int r = 0; r < 8; ++r) {
    bf16x8 A[8], B[8];
#pragma unroll
    for (int p = 0; p < 8; ++p) {
      A[p] = lds_rd16(aaddr + 64 * r + 512 * p);
      const u32x2 lo = *(lds_vu32x2_t)(smem + baddr + 64 * r + 512 * (7 - p));
      const u32x2 hi = *(lds_vu32x2_t)(smem + baddr + 64 * r + 512 * (7 - p) + 8);
      B[p] = __builtin_bit_cast(bf16x8, u32x4{lo[0], lo[1], hi[0], hi[1]});
    }
#pragma unroll
    for (int s = 0; s < 8; ++s)
#pragma unroll
      for (int p = 0; p <= s; ++p)
        acc[s] = __builtin_amdgcn_mfma_f32_16x16x32_bf16(A[p], B[s - p], acc[s], 0, 0, 0);
  }
  STAMP(2);
#pragma unroll
  for (int b = 0; b < 8; ++b) {
    const u32 o = hb + 512 * b + 4 * r16 + 128 * g;
    *(u32*)(smem + o) = pack2(acc[b][0], acc[b][1]);
    *(u32*)(smem + o + 64) = pack2(acc[b][2], acc[b][3]);
  }
  u16* yt = (u16*)(a.ws + WS_YT) + (size_t)ch * S;
#pragma unroll
  for (int i = 0; i < 4; ++i) {
    const int c = i * 64 + lane;
    const u32x4 y = *(const u32x4*)(smem + hb + c * 16);
    const u32x4 uu = *(const u32x4*)(smem + ub + CV_U + (255 - c) * 16);
    u32x4 o;
#pragma unroll
    for (int e = 0; e < 4; ++e)
      o[e] = pack2(__builtin_fmaf(bf_hi(uu[3 - e]), dcoef, bf_lo(y[e])), __builtin_fmaf(bf_lo(uu[3 - e]), dcoef, bf_hi(y[e])));
    *(u32x4*)(yt + c * 8) = o;
  }
  WAIT_VM(0);
  STAMP(3);
  STAMP_OUT(a.ws, 3);
}
constexpr int A_PIECE = 1088, A_BYTES = 16 * A_PIECE;
template <int BN> struct WL;
template <> struct WL<128> { static constexpr int PW = 4, BYTES = 16 * 1056, HI = 256, KK = 8 * 1056; };
template <> struct WL<64> { static constexpr int PW = 2, BYTES = 8 * 1056, HI = 128, KK = 512; };
template <> struct WL<224> { static constexpr int PW = 7, BYTES = 4 * 7200, HI = 4 * 448, KK = 2 * 7200; };

template <int N, class Fn>
__device__ __forceinline__ void static_for(Fn&& f) {
  if constexpr (N > 0) {
    static_for<N - 1>(f);
    f(std::integral_constant<int, N - 1>{});
  }
}

__device__ __forceinline__ u32 gelu2(f32x2 t, f32x2 c0, f32x2 c1, f32x2 sd) {
  const f32x2 z = t * fma2(t * t, c1, c0);
  const f32x2 e = {__builtin_amdgcn_exp2f(z[0]), __builtin_amdgcn_exp2f(z[1])};
  const f32x2 d = fma2(e, sd, sd);
  const f32x2 o = t * f32x2{__builtin_amdgcn_rcpf(d[0]), __builtin_amdgcn_rcpf(d[1])};
  return pack2(o[0], o[1]);
}
__device__ __forceinline__ f32x2 lo2(f32x4 v) { return f32x2{v[0], v[1]}; }
__device__ __forceinline__ f32x2 hi2(f32x4 v) { return f32x2{v[2], v[3]}; }
__device__ __forceinline__ u32x4 swap16(u32 x01, u32 x23, u32 y01, u32 y23) {
  const u32x2 s0 = __builtin_amdgcn_permlane16_swap(x01, y01, false, false);
  const u32x2 s1 = __builtin_amdgcn_permlane16_swap(x23, y23, false, false);
  return u32x4{s0[0], s1[0], s0[1], s1[1]};
}
__device__ __forceinline__ void st16(void* p, u32x4 v) {
  *(u32x4*)p = v;
}

template <int EPI, int BN, int NS, int LA, int RPG>
__device__ __forceinline__ void gemm(const Args& a, int tm, int tn, int cls) {
  constexpr int K = EPI == 4 ? F : D, N = EPI == 3 ? F : D, KT = K / 64;
  constexpr int MT = 4, NT = BN / 32, WTN = BN / 2;
  constexpr int PA = 4, PW = WL<BN>::PW, PER = PA + PW, LAQ = LA / PER, LAR = LA % PER;
  constexpr int STAGE = A_BYTES + WL<BN>::BYTES;
  constexpr int AROWS = EPI == 2 ? S + 4 : S;
  constexpr u32 XTRA = NS * STAGE;
  static_assert(LA <= (NS - 2) * PER, "ring depth");

  STAMP_DECL; STAMP(0);
  const int tid = threadIdx.x, lane = tid & 63;
  const int w8 = __builtin_amdgcn_readfirstlane(tid >> 6);
  const int w = w8 & 3;
  const int m0 = tm * 128, n0 = tn * BN;

  if (w8 >= 4) {
    const void* Ap = EPI == 1 ? (const void*)a.x : (const void*)(a.ws + (EPI == 2 ? WS_H : EPI == 3 ? WS_X1 : WS_G3));
    const u16* Wp = EPI == 1 ? a.w1 : EPI == 2 ? a.w2 : EPI == 3 ? a.w3 : a.w4;
    const __amdgpu_buffer_rsrc_t rA = mk_rsrc(Ap, AROWS * K * 2);
    const __amdgpu_buffer_rsrc_t rW = mk_rsrc(Wp, K * N * 2);
    constexpr int ARS = EPI == 1 ? D * 2 : 128;
    const u32 voffA = (lane >> 3) * ARS + (((lane & 7) ^ (lane >> 4)) * 16);
    u32 voffW[BN == 224 ? 7 : 1];
    if (BN == 224) {
#pragma unroll
      for (int i = 0; i < PW; ++i) {
        const int L = i * 64 + lane, r = L / 28;
        voffW[i] = ((r & 7) + 16 * (r >> 3)) * (N * 2) + (L % 28) * 16;
      }
    } else if (BN == 128) {
      voffW[0] = (4 * ((lane >> 4) & 1) + 16 * (lane >> 5)) * (N * 2) + (lane & 15) * 16;
    } else {
      voffW[0] = (4 * ((lane >> 3) & 1) + 16 * (lane >> 4)) * (N * 2) + (lane & 7) * 16;
    }
    int nxt = (cls == 0) ? 1 : 0;
    u32 sa, sw, lb = 0;
    auto next = [&](int u) {
      int kt;
      if (u == 0) kt = cls;
      else if (((u - 1) & 3) == cls && u + 3 < KT) kt = u + 3;
      else {
        kt = nxt;
        nxt += ((nxt + 1) & 3) == cls ? 2 : 1;
      }
      if (u > 0) lb = (lb + STAGE == NS * STAGE) ? 0 : lb + STAGE;
      int arow = m0 + 8 * w;
      if (EPI == 2) {
        const int grp = kt >> 2;
        arow += 2 + (grp == 0 ? -1 : grp == 1 ? 1 : grp == 2 ? -2 : 2);
      }
      sa = EPI == 1 ? arow * ARS + kt * 128 : (kt * AROWS + arow) * 128;
      sw = (kt * 64 + (BN == 224 ? 8 * (w & 1) + 32 * (w >> 1) : w)) * (N * 2) + n0 * 2;
    };
    auto issue = [&](auto lo, auto hi) {
#pragma unroll
      for (int i = lo; i < hi; ++i) {
        if (i < PW) {
          if (BN == 224) glds16(rW, lb + A_BYTES + w * 7200 + i * 1024, voffW[BN == 224 ? i : 0], sw);
          else glds16(rW, lb + A_BYTES + (4 * i + w) * 1056, voffW[0], sw + (8 * (i & 1) + 32 * (i >> 1)) * (N * 2));
        } else {
          const int p = i - PW;
          glds16(rA, lb + (4 * p + w) * A_PIECE, voffA, sa + 32 * p * ARS);
        }
      }
    };
    using I0 = std::integral_constant<int, 0>;
    using IR = std::integral_constant<int, LAR>;
    using IP = std::integral_constant<int, PER>;
#pragma unroll
    for (int s = 0; s <= LAQ; ++s) {
      next(s);
      issue(I0{}, IP{});
    }
    next(LAQ + 1);
    issue(I0{}, IR{});
    WAIT_VM(LA);
#pragma unroll 1
    for (int kt = 0; kt < KT - LAQ - 2; ++kt) {
      __builtin_amdgcn_s_barrier();
      issue(IR{}, IP{});
      next(kt + LAQ + 2);
      issue(I0{}, IR{});
      WAIT_VM(LA);
    }
    __builtin_amdgcn_s_barrier();
    issue(IR{}, IP{});
    static_for<LAQ + 1>([&](auto z) {
      WAIT_VM((LAQ - z) * PER);
      __builtin_amdgcn_s_barrier();
    });
    return;
  }

  const int wm = w >> 1, wn = w & 1;
  const int r16 = lane & 15, g = lane >> 4;
  const int row0 = m0 + wm * 64 + r16;
  const int col8 = n0 + wn * WTN + 16 * (g & 1) + 8 * (g >> 1);
  u32 ab0 = (wm * 8 + (r16 >> 3)) * A_PIECE + (r16 & 7) * 128 + ((g ^ ((r16 >> 1) & 3)) * 16), ab1;
  u32 wb0 = A_BYTES + wn * WTN * 2 + 8 * (lane & 3), wb1;
  if (BN == 224) wb0 += (g & 1) * 7200 + ((r16 >> 2) + 8 * (g >> 1)) * 448;
  else wb0 += ((r16 >> 2) + 4 * (g & 1)) * 1056 + (g >> 1) * 2 * WL<BN>::HI;
  if (EPI == 1) {
    const int o = wm ^ wn;
    ab1 = ab0 + (o ^ 1) * 64;
    ab0 += o * 64;
    wb1 = wb0 + (o ^ 1) * WL<BN>::KK;
    wb0 += o * WL<BN>::KK;
  }

  u32x4 xr[EPI == 2 || EPI == 4 ? MT : 1][NT / 2], x1r[EPI == 4 ? MT : 1][NT / 2];
  f32x2 xs[MT];
  if (EPI == 2 || EPI == 4) {
#pragma unroll
    for (int i = 0; i < MT; ++i) {
      xs[i] = *(const f32x2*)(a.ws + WS_XS + (size_t)(row0 + 16 * i) * 8);
#pragma unroll
      for (int j = 0; j < NT / 2; ++j) {
        xr[i][j] = *(const u32x4*)(a.x + (size_t)(row0 + 16 * i) * D + col8 + 32 * j);
        if (EPI == 4) {
          const int c = col8 + 32 * j;
          x1r[i][j] = *(const u32x4*)(a.ws + WS_X1 + ((size_t)(c >> 6) * S + row0 + 16 * i) * 128 + (c & 63) * 2);
        }
      }
    }
    if (EPI == 4) {
      const __amdgpu_buffer_rsrc_t rY = mk_rsrc(a.ws + WS_YT, D * S * 2);
#pragma unroll
      for (int i = 0; i < WTN / 8; ++i)
        glds16(rY, XTRA + w * (WTN * 128) + i * 1024, (lane >> 3) * (S * 2) + (((lane & 7) ^ (lane >> 3 & 6)) * 16),
               (n0 + wn * WTN + 8 * i) * (S * 2) + (m0 + wm * 64) * 2);
    }
  }
  if (EPI == 3) {
    float* tab = (float*)(smem + XTRA);
    const float* st = (const float*)(a.ws + WS_ST) + (size_t)(m0 + (tid >> 1)) * 32 + (tid & 1) * 16;
    const float* cs = (const float*)(a.ws + WS_CS) + n0 + (tid < BN ? tid : 0);
    f32x4 v[4];
    float c[4];
#pragma unroll
    for (int i = 0; i < 4; ++i) {
      v[i] = *(const f32x4*)(st + 4 * i);
      c[i] = cs[i * F];
    }
    PIN();
    const f32x4 t = (v[0] + v[1]) + (v[2] + v[3]);
    float s1 = t[0] + t[2], s2 = t[1] + t[3];
    s1 += __shfl_xor(s1, 1, 64);
    s2 += __shfl_xor(s2, 1, 64);
    const float mean = s1 * (1.0f / D);
    const float var = fmaxf(s2 * (1.0f / D) - mean * mean, 0.f);
    tab[tid] = (tid & 1) ? rbf(__builtin_amdgcn_sqrtf(rbf(rbf(var) + 1e-5f))) : rbf(mean);
    if (tid < BN) tab[256 + tid] = (c[0] + c[1]) + (c[2] + c[3]);
  }

  f32x4 acc[MT][NT], sa1[EPI == 1 ? MT : 1], sa2[EPI == 1 ? MT : 1], sw1[EPI == 1 ? NT : 1];
#pragma unroll
  for (int i = 0; i < MT; ++i) {
#pragma unroll
    for (int j = 0; j < NT; ++j) acc[i][j] = f32x4{0.f, 0.f, 0.f, 0.f};
    if (EPI == 1) sa1[i] = sa2[i] = sw1[i] = f32x4{0.f, 0.f, 0.f, 0.f};
  }
  const bf16x8 ones = __builtin_bit_cast(bf16x8, u32x4{0x3F803F80u, 0x3F803F80u, 0x3F803F80u, 0x3F803F80u});

  struct Frag { bf16x8 a[MT]; s16x4 w[NT][2]; };
  constexpr int NR = MT + 2 * NT;
  auto rd = [&](Frag& f, int r, u32 ab, u32 wb) {
    if (r == 2) f.a[0] = lds_rd16(ab);
    else if (r > 2 * NT) f.a[r - 2 * NT] = lds_rd16(ab + (r - 2 * NT) * 2 * A_PIECE);
    else {
      const int z = r > 2 ? r - 1 : r;
      f.w[z >> 1][z & 1] = lds_tr(wb + (z >> 1) * 32 + (z & 1) * WL<BN>::HI);
    }
  };
  auto wfrag = [&](const Frag& f, int j) {
    return __builtin_bit_cast(bf16x8, __builtin_shufflevector(f.w[j][0], f.w[j][1], 0, 1, 2, 3, 4, 5, 6, 7));
  };
  auto half = [&](auto H, const Frag& c, Frag& l, u32 ab, u32 wb, bool load, auto&& late) {
#pragma unroll
    for (int m = 0; m < MT * NT; ++m) {
      const int i = m / NT, j = m % NT;
      acc[i][j] = __builtin_amdgcn_mfma_f32_16x16x32_bf16(wfrag(c, j), c.a[i], acc[i][j], 0, 0, 0);
      if (load) {
#pragma unroll
        for (int r = m * RPG; r < (m + 1) * RPG && r < NR; ++r) rd(l, r, ab, wb);
      }
      if (m == (NR + RPG - 1) / RPG) late();
      PIN();
    }
    if (EPI == 1) {
#pragma unroll
      for (int i = 0; i < MT; ++i) {
        if (H == 0) {
          sa1[i] = __builtin_amdgcn_mfma_f32_16x16x32_bf16(ones, c.a[i], sa1[i], 0, 0, 0);
          sa2[i] = __builtin_amdgcn_mfma_f32_16x16x32_bf16(c.a[i], c.a[i], sa2[i], 0, 0, 0);
        } else {
          sw1[i] = __builtin_amdgcn_mfma_f32_16x16x32_bf16(wfrag(c, i), ones, sw1[i], 0, 0, 0);
        }
      }
      PIN();
    }
  };
  using H0 = std::integral_constant<int, 0>;
  using H1 = std::integral_constant<int, 1>;

  Frag f0, f1;
  WAIT_LGKM0();
  __builtin_amdgcn_s_barrier();
  STAMP(1);
#pragma unroll
  for (int r = 0; r < NR; ++r) rd(f0, r, ab0, wb0);
  PIN();
  int st = 0;
#pragma unroll 1
  for (int kt = 0; kt < KT - 1; ++kt) {
    st = (st + 1 == NS) ? 0 : st + 1;
    const int dl = st == 0 ? -(NS - 1) * STAGE : STAGE;
    half(H0{}, f0, f1, EPI == 1 ? ab1 : ab0 + 64, EPI == 1 ? wb1 : wb0 + WL<BN>::KK, true, [&]() {
      ab0 += dl;
      wb0 += dl;
      asm volatile("" : "+v"(ab0), "+v"(wb0));
    });
    WAIT_LGKM0();
    __builtin_amdgcn_s_barrier();
    PIN();
    half(H1{}, f1, f0, ab0, wb0, true, [&]() {
      if (EPI == 1) {
        ab1 += dl;
        wb1 += dl;
        asm volatile("" : "+v"(ab1), "+v"(wb1));
      }
    });
    WAIT_LGKM0();
  }
  half(H0{}, f0, f1, EPI == 1 ? ab1 : ab0 + 64, EPI == 1 ? wb1 : wb0 + WL<BN>::KK, true, [&]() {});
  WAIT_LGKM0();
  half(H1{}, f1, f0, 0, 0, false, [&]() {});
  STAMP(2);

  if (EPI == 1 || EPI == 3) {
    float* tab = (float*)(smem + XTRA);
    f32x2 mu[MT], c0[MT], c1[MT], sd[MT];
    f32x4 cs[NT];
    if (EPI == 1) {
#pragma unroll
      for (int i = 0; i < MT; ++i) {
        const int r = r16 & 3;
        const float q = r == 0 ? sa2[i][0] : r == 1 ? sa2[i][1] : r == 2 ? sa2[i][2] : sa2[i][3];
        if (g == (r16 >> 2)) *(f32x2*)(tab + w * 128 + (16 * i + r16) * 2) = f32x2{sa1[i][0], q};
        if (r16 == 0) *(f32x4*)(tab + 512 + w * 64 + 16 * i + 4 * g) = sw1[i];
      }
      WAIT_LGKM0();
      __builtin_amdgcn_s_barrier();
#pragma unroll
      for (int i = 0; i < MT; ++i) {
        const f32x2 s = *(const f32x2*)(tab + w * 128 + (16 * i + r16) * 2) + *(const f32x2*)(tab + (w ^ 1) * 128 + (16 * i + r16) * 2);
        cs[i] = sw1[i] + *(const f32x4*)(tab + 512 + (w ^ 2) * 64 + 16 * i + 4 * g);
        const float mean = s[0] * (1.0f / D);
        const float var = fmaxf(s[1] * (1.0f / D) - mean * mean, 0.f);
        const float m1 = rbf(mean), sd1 = rbf(__builtin_amdgcn_sqrtf(rbf(rbf(var) + 1e-5f)));
        const float rs1 = __builtin_amdgcn_rcpf(sd1);
        const float m2 = rbf((mean - m1) * rs1), sd2 = rbf(__builtin_amdgcn_sqrtf(rbf(rbf(var * rs1 * rs1) + 1e-5f)));
        mu[i] = dup2(-__builtin_fmaf(m2, sd1, m1));
        sd[i] = dup2(sd1 * sd2);
        if (tn == 0 && wn == 0 && g == 0) *(f32x2*)(a.ws + WS_XS + (size_t)(row0 + 16 * i) * 8) = f32x2{rs1, -m1 * rs1};
      }
    } else {
#pragma unroll
      for (int i = 0; i < MT; ++i) {
        const f32x2 ms = *(const f32x2*)(tab + 2 * (wm * 64 + 16 * i + r16));
        mu[i] = dup2(-ms[0]);
        sd[i] = dup2(ms[1]);
      }
#pragma unroll
      for (int j = 0; j < NT; ++j) cs[j] = *(const f32x4*)(tab + 256 + wn * WTN + 16 * j + 4 * g);
    }
#pragma unroll
    for (int i = 0; i < MT; ++i) {
      const float rs = __builtin_amdgcn_rcpf(sd[i][0]);
      c0[i] = dup2(-2.302208198f * rs);
      c1[i] = dup2(-0.1029432397f * rs * rs * rs);
    }
    char* op = a.ws + (EPI == 1 ? WS_H + 2 * 128 : WS_G3);
    constexpr int OROWS = EPI == 1 ? S + 4 : S;
#pragma unroll
    for (int i = 0; i < MT; ++i) {
#pragma unroll
      for (int j = 0; j < NT; j += 2) {
        const bool last = (j + 1 == NT);
        if (last && (i & 1)) continue;
        const int i2 = last ? i + 1 : i, j2 = last ? j : j + 1;
        const u32 x01 = gelu2(fma2(lo2(cs[j]), mu[i], lo2(acc[i][j])), c0[i], c1[i], sd[i]);
        const u32 x23 = gelu2(fma2(hi2(cs[j]), mu[i], hi2(acc[i][j])), c0[i], c1[i], sd[i]);
        const u32 y01 = gelu2(fma2(lo2(cs[j2]), mu[i2], lo2(acc[i2][j2])), c0[i2], c1[i2], sd[i2]);
        const u32 y23 = gelu2(fma2(hi2(cs[j2]), mu[i2], hi2(acc[i2][j2])), c0[i2], c1[i2], sd[i2]);
        const int row = row0 + 16 * i + (last ? 16 * (g & 1) : 0);
        const int col = col8 + 16 * j - (last ? 16 * (g & 1) : 0);
        st16(op + ((size_t)(col >> 6) * OROWS + row) * 128 + (col & 63) * 2, swap16(x01, x23, y01, y23));
      }
    }
  } else if (EPI == 2) {
#pragma unroll
    for (int i = 0; i < MT; ++i) {
      f32x2 s1 = {0.f, 0.f}, s2 = {0.f, 0.f};
#pragma unroll
      for (int j = 0; j < NT; j += 2) {
        const u32x4 xv = swap16(xr[i][j / 2][0], xr[i][j / 2][1], xr[i][j / 2][2], xr[i][j / 2][3]);
        u32 o[4];
#pragma unroll
        for (int e = 0; e < 4; ++e) {
          const f32x4 A = acc[i][j + (e >> 1)];
          const f32x2 v = fma2(bf_2(xv[e]), dup2(xs[i][0]), ((e & 1) ? hi2(A) : lo2(A)) + dup2(xs[i][1]));
          s1 += v;
          s2 = fma2(v, v, s2);
          o[e] = pack2(v[0], v[1]);
        }
        const int col = col8 + 16 * j;
        st16(a.ws + WS_X1 + ((size_t)(col >> 6) * S + row0 + 16 * i) * 128 + (col & 63) * 2, swap16(o[0], o[1], o[2], o[3]));
      }
      float t1 = s1[0] + s1[1], t2 = s2[0] + s2[1];
      t1 += __shfl_xor(t1, 16, 64);
      t2 += __shfl_xor(t2, 16, 64);
      t1 += __shfl_xor(t1, 32, 64);
      t2 += __shfl_xor(t2, 32, 64);
      if (g == 0) *(f32x2*)(a.ws + WS_ST + (size_t)(row0 + 16 * i) * 128 + (tn * 2 + wn) * 8) = f32x2{t1, t2};
    }
  } else {
    WAIT_VM(0);
#pragma unroll
    for (int i = 0; i < MT; ++i) {
#pragma unroll
      for (int j = 0; j < NT; j += 2) {
        const u32x4 xv = swap16(xr[i][j / 2][0], xr[i][j / 2][1], xr[i][j / 2][2], xr[i][j / 2][3]);
        const u32x4 x1v = swap16(x1r[i][j / 2][0], x1r[i][j / 2][1], x1r[i][j / 2][2], x1r[i][j / 2][3]);
        u32 o[4];
#pragma unroll
        for (int jj = 0; jj < 2; ++jj) {
          const int dr = 16 * (j + jj) + 4 * g + (r16 >> 2);
          const u32x2 yv = __builtin_bit_cast(u32x2, lds_tr(XTRA + w * (WTN * 128) + dr * 128 + ((i ^ (dr >> 1 & 3)) * 32) + 8 * (lane & 3)));
#pragma unroll
          for (int e = 0; e < 2; ++e) {
            const f32x2 xf = bf_2(xv[2 * jj + e]);
            const f32x2 ob = (e ? hi2(acc[i][j + jj]) : lo2(acc[i][j + jj])) + bf_2(x1v[2 * jj + e]);
            const f32x2 z = ob * dup2(-1.442695041f);
            const f32x2 d = f32x2{__builtin_amdgcn_exp2f(z[0]), __builtin_amdgcn_exp2f(z[1])} + dup2(1.0f);
            const f32x2 gt = ob * f32x2{__builtin_amdgcn_rcpf(d[0]), __builtin_amdgcn_rcpf(d[1])};
            const f32x2 sv = fma2(xf, dup2(xs[i][0]), dup2(xs[i][1])) * bf_2(yv[e]);
            const f32x2 r = fma2(sv, gt, xf);
            o[2 * jj + e] = pack2(r[0], r[1]);
          }
        }
        *(u32x4*)(a.out + (size_t)(row0 + 16 * i) * D + col8 + 16 * j) = swap16(o[0], o[1], o[2], o[3]);
      }
    }
  }
  STAMP(3);
  STAMP_OUT(a.ws, EPI <= 2 ? EPI : EPI + 1);
}

#define LA1 12
#define LA3 11
#define LA4 9
#define NS4 4
constexpr int LDS_BYTES = 147456;

__global__ __launch_bounds__(512) void k1(Args a) {
  const int xcd = blockIdx.x & 7, idx = blockIdx.x >> 3;
  if (idx < 16) gemm<1, 128, 4, LA1, 2>(a, (xcd >> 1) * 4 + (idx >> 2), (xcd & 1) * 4 + (idx & 3), ((idx >> 2) + idx) & 3);
  else side1(a, xcd, idx - 16);
}
__global__ __launch_bounds__(512) void k2(Args a) {
  const int xcd = blockIdx.x & 7, idx = blockIdx.x >> 3;
  if (idx < 16) gemm<2, 128, 4, LA1, 2>(a, (xcd >> 1) * 4 + (idx >> 2), (xcd & 1) * 4 + (idx & 3), ((idx >> 2) + idx) & 3);
  else conv(a, xcd * 16 + idx - 16);
}
__global__ __launch_bounds__(512) void k3(Args a) {
  const int xcd = blockIdx.x & 7, idx = blockIdx.x >> 3;
  gemm<3, 224, 3, LA3, 1>(a, (xcd >> 2) * 8 + (idx >> 2), (xcd & 3) * 4 + (idx & 3), ((idx >> 2) + idx) & 3);
}
__global__ __launch_bounds__(512) void k4(Args a) {
  const int xcd = blockIdx.x & 7, idx = blockIdx.x >> 3;
  gemm<4, 64, NS4, LA4, 2>(a, (xcd >> 1) * 4 + (idx >> 3), (xcd & 1) * 8 + (idx & 7), ((idx >> 3) + idx) & 3);
}

extern "C" void kernel_launch(void* const* d_in, const int* in_sizes, int n_in, void* d_out, int out_size, void* d_ws,
                              size_t ws_size, hipStream_t stream) {
  Args a;
  a.x = (const u16*)d_in[0];
  a.k = (const u16*)d_in[1];
  a.v = (const u16*)d_in[2];
  a.shk = (const u16*)d_in[3];
  a.ssm_k = (const u16*)d_in[4];
  a.ssm_D = (const u16*)d_in[5];
  a.w1 = (const u16*)d_in[7];
  a.w2 = (const u16*)d_in[8];
  a.w3 = (const u16*)d_in[9];
  a.w4 = (const u16*)d_in[10];
  a.ws = (char*)d_ws;
  a.out = (u16*)d_out;
  (void)hipFuncSetAttribute((const void*)k1, hipFuncAttributeMaxDynamicSharedMemorySize, LDS_BYTES);
  (void)hipFuncSetAttribute((const void*)k2, hipFuncAttributeMaxDynamicSharedMemorySize, LDS_BYTES);
  (void)hipFuncSetAttribute((const void*)k3, hipFuncAttributeMaxDynamicSharedMemorySize, LDS_BYTES);
  (void)hipFuncSetAttribute((const void*)k4, hipFuncAttributeMaxDynamicSharedMemorySize, LDS_BYTES);
  k1<<<256, 512, LDS_BYTES, stream>>>(a);
  k2<<<256, 512, LDS_BYTES, stream>>>(a);
  k3<<<256, 512, LDS_BYTES, stream>>>(a);
  k4<<<256, 512, LDS_BYTES, stream>>>(a);
}
```

```cpp
#include <hip/hip_runtime.h>
#include <hip/hip_bf16.h>
#include <stdint.h>
#pragma clang diagnostic ignored "-Wint-to-pointer-cast"

typedef unsigned short u16;
typedef unsigned int u32;
typedef unsigned long long u64;
typedef __attribute__((ext_vector_type(8))) __bf16 bf16x8;
typedef __attribute__((ext_vector_type(4))) short s16x4;
typedef __attribute__((ext_vector_type(4))) float f32x4;
typedef __attribute__((ext_vector_type(2))) float f32x2;
typedef __attribute__((ext_vector_type(2))) u32 u32x2;
typedef __attribute__((ext_vector_type(4))) u32 u32x4;
typedef __attribute__((ext_vector_type(2))) __bf16 bf16x2;
typedef __attribute__((address_space(1))) u64 gu64;
typedef __amdgpu_buffer_rsrc_t rsrc_t;
#define AS3 __attribute__((address_space(3)))
#define LDS(T, off) (*(AS3 T*)(u32)(off))

constexpr int S = 2048, D = 1024, F = 3584;

constexpr u32 WS_FL = 1024;
constexpr u32 F_S1 = 0, F_KV = 128, F_C = 256, F_HE = 384, F_HL = 512, F_X1 = 640, F_ST = 768, F_GE = 896, F_GL = 1152, F_EX = 1408;
constexpr u32 WS_XS = WS_FL + 1664 * 128;
constexpr u32 WS_C1 = WS_XS + 2 * (S + 256) * 16;
constexpr u32 WS_C3 = WS_C1 + 5 * D * 4;
constexpr u32 WS_ST = WS_C3 + 4 * F * 4;
constexpr u32 WS_H = 1u << 20;
constexpr u32 WS_X1 = WS_H + S * D * 2;
constexpr u32 WS_G3 = WS_X1 + S * D * 2;
constexpr u32 WS_UR = WS_G3 + S * F * 2;
constexpr u32 WS_YT = WS_UR + D * S * 2;
constexpr u32 WS_EX = WS_YT + D * S * 2;
constexpr u32 WS_END = WS_EX + 256 * 32768;
static_assert(WS_ST + 16 * 16 * 128 * 8 <= WS_H, "ws");

constexpr int CV_H = 4576, CV_U = 4192, CV_CH = 2 * CV_H + 2 * CV_U;
constexpr u32 L_TB = 8 * CV_CH;
constexpr u32 L_T3 = L_TB + 16384;
constexpr u32 L_DUM = L_T3 + 3072;
constexpr u32 L_CNT = L_DUM + 2048;
constexpr int LDS_BYTES = 163840;
static_assert(L_CNT + 64 <= LDS_BYTES, "lds");

__device__ __forceinline__ float bf_lo(u32 v) { return __uint_as_float(v << 16); }
__device__ __forceinline__ float bf_hi(u32 v) { return __uint_as_float(v & 0xffff0000u); }
__device__ __forceinline__ f32x2 bf_2(u32 v) { return f32x2{bf_lo(v), bf_hi(v)}; }
__device__ __forceinline__ u32 pack2(float lo, float hi) {
  bf16x2 r;
  r[0] = (__bf16)lo;
  r[1] = (__bf16)hi;
  return __builtin_bit_cast(u32, r);
}
__device__ __forceinline__ float rbf(float x) { return (float)(__bf16)x; }
__device__ __forceinline__ f32x2 fma2(f32x2 a, f32x2 b, f32x2 c) { return __builtin_elementwise_fma(a, b, c); }
__device__ __forceinline__ f32x2 dup2(float a) { return f32x2{a, a}; }
__device__ __forceinline__ f32x2 lo2(f32x4 v) { return f32x2{v[0], v[1]}; }
__device__ __forceinline__ f32x2 hi2(f32x4 v) { return f32x2{v[2], v[3]}; }
__device__ __forceinline__ float dot2(u32 a, u32 b, float c) {
  return __builtin_amdgcn_fdot2_f32_bf16(__builtin_bit_cast(bf16x2, a), __builtin_bit_cast(bf16x2, b), c, false);
}
__device__ __forceinline__ u32 sconst(u32 v) {
  asm("" : "+s"(v));
  return v;
}
template <int CTRL>
__device__ __forceinline__ float dpp_add(float v) {
  return v + __int_as_float(__builtin_amdgcn_update_dpp(0, __float_as_int(v), CTRL, 0xf, 0xf, true));
}
__device__ __forceinline__ float red32(float a, float b) {
  const u32x2 s = __builtin_amdgcn_permlane32_swap(__float_as_uint(a), __float_as_uint(b), false, false);
  return __uint_as_float(s[0]) + __uint_as_float(s[1]);
}
__device__ __forceinline__ float red16(float a, float b) {
  const u32x2 s = __builtin_amdgcn_permlane16_swap(__float_as_uint(a), __float_as_uint(b), false, false);
  return __uint_as_float(s[0]) + __uint_as_float(s[1]);
}
__device__ __forceinline__ u32x4 swap16(u32 x01, u32 x23, u32 y01, u32 y23) {
  const u32x2 s0 = __builtin_amdgcn_permlane16_swap(x01, y01, false, false);
  const u32x2 s1 = __builtin_amdgcn_permlane16_swap(x23, y23, false, false);
  return u32x4{s0[0], s1[0], s0[1], s1[1]};
}

#define STAMP(i)
#define STAMP_DECL
#define STAMP_OUT(ws, kid)
#define WAIT_VM(n) asm volatile("s_waitcnt vmcnt(%0)" ::"n"(n) : "memory")
#define WAIT_LGKM0() __builtin_amdgcn_s_waitcnt(0xC07F)
#define PIN() __builtin_amdgcn_sched_barrier(0)
#define BAR() __builtin_amdgcn_s_barrier()

__device__ __forceinline__ rsrc_t mk_rsrc(const void* p, u32 bytes) {
  return __builtin_amdgcn_make_buffer_rsrc((void*)p, 0, bytes, 0x00020000);
}
template <bool SC1>
__device__ __forceinline__ void glds16(rsrc_t r, u32 lds_off, u32 voff, u32 soff) {
  if (SC1)
    asm volatile("s_mov_b32 m0, %0\n\ts_nop 0\n\tbuffer_load_dwordx4 %1, %2, %3 offen sc1 lds" ::"s"(lds_off), "v"(voff), "s"(r), "s"(soff) : "memory");
  else
    asm volatile("s_mov_b32 m0, %0\n\ts_nop 0\n\tbuffer_load_dwordx4 %1, %2, %3 offen lds" ::"s"(lds_off), "v"(voff), "s"(r), "s"(soff) : "memory");
}
__device__ __forceinline__ void touch(rsrc_t r, u32 lds_off, u32 voff, u32 soff) {
  asm volatile("s_mov_b32 m0, %0\n\ts_nop 0\n\tbuffer_load_dword %1, %2, %3 offen lds" ::"s"(lds_off), "v"(voff), "s"(r), "s"(soff) : "memory");
}
__device__ __forceinline__ s16x4 lds_tr(u32 off) { return __builtin_amdgcn_ds_read_tr16_b64_v4i16((AS3 s16x4*)off); }
__device__ __forceinline__ void st16(rsrc_t r, u32 voff, u32 soff, u32x4 v) { __builtin_amdgcn_raw_buffer_store_b128(v, r, voff + soff, 0, 16); }
__device__ __forceinline__ u32x4 ld16(rsrc_t r, u32 voff, u32 soff) { return __builtin_amdgcn_raw_buffer_load_b128(r, voff, soff, 16); }

struct Args {
  const u16 *x, *k, *v, *shk, *ssm_k, *ssm_D, *w1, *w2, *w3, *w4;
  char* ws;
  u16* out;
};

struct Ctx {
  rsrc_t ws;
  u64 tag;
  int lane, w;
};
__device__ __forceinline__ int fresh(int v) {
  asm volatile("" : "+v"(v));
  return v;
}
__device__ __forceinline__ void set_flag(const Ctx& c, u32 line, u32 sub) {
  __builtin_amdgcn_raw_buffer_store_b64(u32x2{(u32)c.tag, (u32)(c.tag >> 32)}, c.ws, c.lane == 0 ? 0u : 0x80000000u, WS_FL + line * 128 + sub * 32, 16);
}
__device__ __forceinline__ u32 foff(u32 line, u32 sub) { return WS_FL + line * 128 + sub * 32; }
__device__ __forceinline__ void acq() {
  asm volatile("" ::: "memory");
}
__device__ __forceinline__ void spin(const Args& a, const Ctx& c, u32 off) {
#pragma unroll 1
  for (int n = 0; n < (1 << 14); ++n) {
    const u64 v = __hip_atomic_load((gu64*)(a.ws + off), __ATOMIC_RELAXED, __HIP_MEMORY_SCOPE_AGENT);
    if (__builtin_amdgcn_ballot_w64(v != c.tag) == 0) break;
    __builtin_amdgcn_s_sleep(2);
  }
}
__device__ __forceinline__ void poll(const Args& a, const Ctx& c, u32 off) {
  spin(a, c, off);
  acq();
}
__device__ __forceinline__ u32x2 peek(const Ctx& c, u32 off) {
  u32x2 v;
  asm volatile("buffer_load_dwordx2 %0, %1, %2, 0 offen sc1" : "=v"(v) : "v"(off), "s"(c.ws) : "memory");
  return v;
}
template <int N>
__device__ __forceinline__ void check(const Args& a, const Ctx& c, u32x2 v, u32 off) {
  asm volatile("s_waitcnt vmcnt(%1)" : "+v"(v) : "n"(N) : "memory");
  if (__builtin_amdgcn_ballot_w64(__builtin_bit_cast(u64, v) != c.tag) != 0) spin(a, c, off);
  acq();
}
__device__ __forceinline__ u32 arrive(const Ctx& c, u32 cnt) {
  u32 old = 0;
  if (c.lane == 0) old = __hip_atomic_fetch_add((AS3 u32*)(L_CNT + 4 * cnt), 1u, __ATOMIC_RELAXED, __HIP_MEMORY_SCOPE_WORKGROUP);
  return __builtin_amdgcn_readfirstlane(old);
}
__device__ __forceinline__ void soft_wait(u32 cnt) {
#pragma unroll 1
  for (int n = 0; n < (1 << 19); ++n) {
    const u32 v = __hip_atomic_load((AS3 u32*)(L_CNT + 4 * cnt), __ATOMIC_RELAXED, __HIP_MEMORY_SCOPE_WORKGROUP);
    if (__builtin_amdgcn_readfirstlane(v) == 8) break;
  }
  asm volatile("" ::: "memory");
}
#define TAG_WAIT(c) do { PIN(); (c).tag += 1; asm volatile("s_waitcnt lgkmcnt(0)" : "+s"((c).tag) :: "memory"); } while (0)

__device__ __forceinline__ void side_stats(const Args& a, Ctx& c, int xcd, int j16) {
  STAMP_DECL; STAMP(0);
  const int lane = c.lane, w = c.w;
  const int row0 = (xcd >> 1) * 512 + j16 * 32, col0 = (xcd & 1) * 512 + j16 * 32;
  const u16* xp = a.x + (size_t)(row0 + 8 * w + (lane >> 3)) * D + 8 * (lane & 7);
  const u16* wp = a.w1 + (size_t)(16 * w + (lane >> 2)) * D + col0 + 8 * (lane & 3);
  u32x4 xr[16], wr[16];
#pragma unroll
  for (int i = 0; i < 16; ++i) {
    xr[i] = *(const u32x4*)(xp + 64 * i);
    wr[i] = *(const u32x4*)(wp + (size_t)64 * i * D);
  }
  PIN();
  if (w == 0 && lane < 4) LDS(u32, L_CNT + 4 * lane) = 0u;
  TAG_WAIT(c);
  BAR();
  PIN();
  const u32 o11 = sconst(0x3F803F80u), o01 = sconst(0x00003F80u), o10 = sconst(0x3F800000u);
  float s1 = 0.f, s2 = 0.f, cs[8];
#pragma unroll
  for (int e = 0; e < 8; ++e) cs[e] = 0.f;
#pragma unroll
  for (int i = 0; i < 16; ++i) {
#pragma unroll
    for (int e = 0; e < 4; ++e) {
      s1 = dot2(xr[i][e], o11, s1);
      s2 = dot2(xr[i][e], xr[i][e], s2);
      cs[2 * e] = dot2(wr[i][e], o01, cs[2 * e]);
      cs[2 * e + 1] = dot2(wr[i][e], o10, cs[2 * e + 1]);
    }
    PIN();
  }
  STAMP(1);
  s1 = dpp_add<0x141>(dpp_add<0x4E>(dpp_add<0xB1>(s1)));
  s2 = dpp_add<0x141>(dpp_add<0x4E>(dpp_add<0xB1>(s2)));
#pragma unroll
  for (int e = 0; e < 8; ++e) cs[e] = dpp_add<0x128>(dpp_add<0x124>(cs[e]));
  if ((lane & 12) == 0) {
    const u32 o = L_TB + 1024 + (w * 4 + (lane >> 4)) * 128 + (lane & 3) * 32;
    LDS(f32x4, o) = f32x4{cs[0], cs[1], cs[2], cs[3]};
    LDS(f32x4, o + 16) = f32x4{cs[4], cs[5], cs[6], cs[7]};
  }
  const float mean = s1 * (1.0f / D);
  const float var = fmaxf(s2 * (1.0f / D) - mean * mean, 0.f);
  const float m1 = rbf(mean), sd1 = rbf(__builtin_amdgcn_sqrtf(rbf(rbf(var) + 1e-5f)));
  const float rs1 = __builtin_amdgcn_rcpf(sd1);
  const float m2 = rbf((mean - m1) * rs1), sd2 = rbf(__builtin_amdgcn_sqrtf(rbf(rbf(var * rs1 * rs1) + 1e-5f)));
  if ((lane & 7) == 0) LDS(f32x4, L_TB + (8 * w + (lane >> 3)) * 16) = f32x4{rs1, -m1 * rs1, -__builtin_fmaf(m2, sd1, m1), sd1 * sd2};
  asm volatile("" ::: "memory");
  if (arrive(c, 0) == 3) {
    asm volatile("" ::: "memory");
    if (lane < 32) {
      float t = 0.f;
#pragma unroll
      for (int i = 0; i < 16; ++i) t += LDS(float, L_TB + 1024 + i * 128 + lane * 4);
      st16(c.ws, lane * 16, WS_XS + ((xcd & 1) * (S + 256) + 64 + row0) * 16, LDS(u32x4, L_TB + lane * 16));
      __builtin_amdgcn_raw_buffer_store_b32(__float_as_uint(t), c.ws, lane * 4, WS_C1 + ((xcd >> 1) * D + col0) * 4, 16);
    }
    WAIT_VM(0);
    set_flag(c, F_S1 + xcd * 16 + j16, 0);
  }
  STAMP(2);
  touch(mk_rsrc(a.ssm_k, D * S * 2), L_DUM + w * 256, lane * 64, ((xcd * 16 + j16) * 8 + 4 + w) * 4096);
  STAMP(3);
  STAMP_OUT(a.ws, 0);
}

#define KV_SLEEP 40
__device__ __forceinline__ void side_kv(const Args& a, Ctx& c, int xcd, int j16) {
  STAMP_DECL; STAMP(0);
  const int lane = c.lane, w = c.w;
  BAR();
  __builtin_amdgcn_s_sleep(KV_SLEEP);
  const int t0 = j16 * 128, d0 = xcd * 128 + w * 32;
  const int tg = lane >> 2, dc = lane & 3;
  const int tb = t0 + 8 * tg, dd = d0 + 8 * dc;
  u32x4 kr[11], vr[8], sh[4];
#pragma unroll
  for (int i = 0; i < 4; ++i) sh[i] = *(const u32x4*)(a.shk + (size_t)dd * 4 + 8 * i);
#pragma unroll
  for (int i = 3; i < 11; ++i) kr[i] = *(const u32x4*)(a.k + (size_t)(tb - 3 + i) * D + dd);
#pragma unroll
  for (int i = 0; i < 8; ++i) vr[i] = *(const u32x4*)(a.v + (size_t)(tb + i) * D + dd);
#pragma unroll
  for (int i = 0; i < 3; ++i) kr[i] = *(const u32x4*)(a.k + (size_t)(tb == 0 ? 0 : tb - 3 + i) * D + dd);
  PIN();
  TAG_WAIT(c);
  if (tb == 0) {
#pragma unroll
    for (int i = 0; i < 3; ++i) kr[i] = u32x4{0u, 0u, 0u, 0u};
  }
  const u32 T = (w + 4) * CV_CH + 2 * CV_H;
#pragma unroll
  for (int e = 0; e < 4; ++e) {
    const f32x2 c0 = {bf_lo(sh[e][0]), bf_lo(sh[e][2])}, c1 = {bf_hi(sh[e][0]), bf_hi(sh[e][2])};
    const f32x2 c2 = {bf_lo(sh[e][1]), bf_lo(sh[e][3])}, c3 = {bf_hi(sh[e][1]), bf_hi(sh[e][3])};
    f32x2 kf[11], o[8];
#pragma unroll
    for (int i = 0; i < 11; ++i) kf[i] = bf_2(kr[i][e]);
#pragma unroll
    for (int i = 0; i < 8; ++i) o[i] = fma2(kf[i + 3], c0, fma2(kf[i + 2], c1, fma2(kf[i + 1], c2, kf[i] * c3))) * bf_2(vr[i][e]);
#pragma unroll
    for (int h = 0; h < 2; ++h)
      LDS(u32x4, T + (8 * dc + 2 * e + h) * 256 + (((15 - tg) ^ (2 * dc)) * 16)) =
          u32x4{pack2(o[7][h], o[6][h]), pack2(o[5][h], o[4][h]), pack2(o[3][h], o[2][h]), pack2(o[1][h], o[0][h])};
  }
  STAMP(1);
#pragma unroll
  for (int q = 0; q < 8; ++q)
    st16(c.ws, (lane >> 4) * (S * 2) + (lane & 15) * 16, WS_UR + (d0 + 4 * q) * (S * 2) + (S - 128 - t0) * 2,
         LDS(u32x4, T + (4 * q + (lane >> 4)) * 256 + (((lane & 15) ^ (q & 6)) * 16)));
  STAMP(2);
  STAMP(3);
  STAMP_OUT(a.ws, 6);
}

#define CV_SLEEP 8
__device__ __forceinline__ void conv(const Args& a, const Ctx& c, int w8, int xcd, int j16) {
  STAMP_DECL; STAMP(0);
  const int lane = fresh(c.lane);
  const int u = xcd * 16 + j16, ch = u * 8 + w8;
  const u32 hb = w8 * CV_CH, ub = hb + 2 * CV_H;
  const u16* hrow = a.ssm_k + (size_t)ch * S;
  u32x4 hc[4];
  u32 hp[4];
#pragma unroll
  for (int i = 0; i < 4; ++i) {
    const int n = i * 64 + lane;
    hc[i] = *(const u32x4*)(hrow + 8 * n);
    hp[i] = *(const u32*)(hrow + (n > 0 ? 8 * n - 2 : 0));
  }
  const float dcoef = bf_lo((u32)a.ssm_D[ch]);
  PIN();
  if (w8 >= 4) {
    WAIT_VM(8);
    set_flag(c, F_KV + u, w8 - 4);
  }
  {
    const u32 zo = lane < 28 ? hb + lane * 16 : lane < 56 ? hb + CV_H + (lane - 28) * 16 : ub + ((lane >> 2) & 1) * CV_U + 4096 + (lane & 3) * 16;
    LDS(u32x4, zo) = u32x4{0u, 0u, 0u, 0u};
  }
  if (lane == 0) hp[0] = 0u;
#pragma unroll
  for (int i = 0; i < 4; ++i) {
    u32x4 o;
    o[0] = __builtin_amdgcn_alignbit(hc[i][0], hp[i], 16);
    o[1] = __builtin_amdgcn_alignbit(hc[i][1], hc[i][0], 16);
    o[2] = __builtin_amdgcn_alignbit(hc[i][2], hc[i][1], 16);
    o[3] = __builtin_amdgcn_alignbit(hc[i][3], hc[i][2], 16);
    LDS(u32x4, hb + 448 + (i * 64 + lane) * 16) = o;
    LDS(u32x4, hb + CV_H + 448 + (i * 64 + lane) * 16) = hc[i];
  }
  if (w8 == 0) poll(a, c, foff(F_KV + xcd * 16 + (lane >> 2), lane & 3));
  WAIT_LGKM0();
  BAR();
#pragma unroll
  for (int i = 0; i < 4; ++i) {
    glds16<true>(c.ws, ub + CV_U + i * 1024, lane * 16, WS_UR + ch * (S * 2) + i * 1024);
    glds16<true>(c.ws, ub + i * 1024, lane * 16, WS_UR + ch * (S * 2) + i * 1024 + 4);
  }
  const int jc = j16 < 14 ? j16 : 13, rb = (xcd >> 2) * 2 + (jc & 1), cb = (xcd & 3) * 896 + (jc >> 1) * 128;
  u32x4 wr[8];
#pragma unroll
  for (int i = 0; i < 8; ++i) wr[i] = *(const u32x4*)(a.w3 + (size_t)(rb * 256 + 4 * w8 + (lane >> 4) + 32 * i) * F + cb + 8 * (lane & 15));
  PIN();
  {
    const rsrc_t r3 = mk_rsrc(a.w3, D * F * 2), r4 = mk_rsrc(a.w4, F * D * 2);
    const int l = lane < 56 ? lane : 55;
#pragma unroll
    for (int i = 0; i < 2; ++i) {
      touch(r3, L_DUM + w8 * 256, (l / 28) * (F * 2) + (l % 28) * 64, (((xcd >> 2) ^ 1) * 512 + j16 * 32 + w8 * 4 + 2 * i) * (F * 2) + (xcd & 3) * 1792);
      touch(r4, L_DUM + w8 * 256, l * 64, ch * 7168 + i * 3584);
    }
  }
  WAIT_VM(12);
  if (lane == 0) LDS(u32, ub + 4092) = 0u;
  if (w8 >= 4) __builtin_amdgcn_s_sleep(CV_SLEEP);
  STAMP(1);
  const int r16 = lane & 15, g = lane >> 4;
  const u32 aaddr = hb + (r16 & 1) * CV_H + 64 * (r16 >> 1) + 16 * g;
  const u32 baddr = ub + (r16 & 1) * CV_U + 56 - 8 * (r16 >> 1) + 16 * g;
  f32x4 acc[8];
#pragma unroll
  for (int b = 0; b < 8; ++b) acc[b] = f32x4{0.f, 0.f, 0.f, 0.f};
#pragma unroll 1
  for (int r = 0; r < 8; ++r) {
    bf16x8 A[8], B[8];
#pragma unroll
    for (int p = 0; p < 8; ++p) {
      A[p] = LDS(bf16x8, aaddr + 64 * r + 512 * p);
      const u32x2 lo = LDS(volatile u32x2, baddr + 64 * r + 512 * (7 - p));
      const u32x2 hi = LDS(volatile u32x2, baddr + 64 * r + 512 * (7 - p) + 8);
      B[p] = __builtin_bit_cast(bf16x8, u32x4{lo[0], lo[1], hi[0], hi[1]});
    }
#pragma unroll
    for (int s = 0; s < 8; ++s)
#pragma unroll
      for (int p = 0; p <= s; ++p) acc[s] = __builtin_amdgcn_mfma_f32_16x16x32_bf16(A[p], B[s - p], acc[s], 0, 0, 0);
  }
  STAMP(2);
#pragma unroll
  for (int b = 0; b < 8; ++b) {
    const u32 o = hb + 512 * b + 4 * r16 + 128 * g;
    LDS(u32, o) = pack2(acc[b][0], acc[b][1]);
    LDS(u32, o + 64) = pack2(acc[b][2], acc[b][3]);
  }
#pragma unroll
  for (int i = 0; i < 4; ++i) {
    const int n = i * 64 + lane;
    const u32x4 y = LDS(u32x4, hb + n * 16);
    const u32x4 uu = LDS(u32x4, ub + CV_U + (255 - n) * 16);
    u32x4 o;
#pragma unroll
    for (int e = 0; e < 4; ++e)
      o[e] = pack2(__builtin_fmaf(bf_hi(uu[3 - e]), dcoef, bf_lo(y[e])), __builtin_fmaf(bf_lo(uu[3 - e]), dcoef, bf_hi(y[e])));
    st16(c.ws, lane * 16, WS_YT + ch * (S * 2) + i * 1024, o);
  }
  PIN();
  {
    const u32 o01 = sconst(0x00003F80u), o10 = sconst(0x3F800000u);
    float cs[8];
#pragma unroll
    for (int e = 0; e < 8; ++e) {
      cs[e] = 0.f;
#pragma unroll
      for (int i = 0; i < 8; ++i) cs[e] = dot2(wr[i][e >> 1], (e & 1) ? o10 : o01, cs[e]);
    }
    const float u0 = red32(cs[0], cs[4]), u1 = red32(cs[1], cs[5]), u2 = red32(cs[2], cs[6]), u3 = red32(cs[3], cs[7]);
    LDS(f32x2, L_TB + w8 * 512 + r16 * 32 + g * 8) = f32x2{red16(u0, u2), red16(u1, u3)};
  }
  WAIT_LGKM0();
  BAR();
  if (w8 < 2 && j16 < 14) {
    float t = 0.f;
#pragma unroll
    for (int i = 0; i < 8; ++i) t += LDS(float, L_TB + i * 512 + w8 * 256 + lane * 4);
    __builtin_amdgcn_raw_buffer_store_b32(__float_as_uint(t), c.ws, lane * 4, WS_C3 + (rb * F + cb + w8 * 64) * 4, 16);
  }
  STAMP(3);
  STAMP_OUT(a.ws, 3);
}

template <int BN> struct WL;
template <> struct WL<128> { static constexpr int BYTES = 16 * 1056, HI = 256, KK = 8 * 1056; };
template <> struct WL<224> { static constexpr int BYTES = 4 * 7200, HI = 4 * 448, KK = 2 * 7200; };
constexpr int AP1 = 1088, AB1 = 16 * AP1, AB = 16384;
constexpr int ST1 = AB1 + WL<128>::BYTES, ST2 = AB + WL<128>::BYTES, ST3 = AB + WL<224>::BYTES;
static_assert(4 * ST1 <= (int)L_TB && 3 * ST3 <= (int)L_TB, "ring");
#define TAIL 8
constexpr int NFS = 28 - TAIL / 2;

template <int NT> struct Frag { bf16x8 a[4]; s16x4 w[NT][2]; };

template <int BN, int MT, int AI>
__device__ __forceinline__ void rd(Frag<BN / 32>& f, int r, u32 ab, u32 wb) {
  constexpr int NT = BN / 32;
  if (r == 2) f.a[0] = LDS(bf16x8, ab);
  else if (r > 2 * NT) f.a[r - 2 * NT] = LDS(bf16x8, ab + (r - 2 * NT) * AI);
  else {
    const int z = r > 2 ? r - 1 : r;
    f.w[z >> 1][z & 1] = lds_tr(wb + (z >> 1) * 32 + (z & 1) * WL<BN>::HI);
  }
}
template <int BN, int MT, int AI, bool LOAD, class Late>
__device__ __forceinline__ void half(f32x4 (&acc)[4][BN / 32], const Frag<BN / 32>& c, Frag<BN / 32>& l, u32 ab, u32 wb, Late&& late) {
  constexpr int NT = BN / 32, NR = MT + 2 * NT, RPG = MT == 4 ? 1 : 2;
#pragma unroll
  for (int m = 0; m < MT * NT; ++m) {
    const int i = m / NT, j = m % NT;
    acc[i][j] = __builtin_amdgcn_mfma_f32_16x16x32_bf16(
        __builtin_bit_cast(bf16x8, __builtin_shufflevector(c.w[j][0], c.w[j][1], 0, 1, 2, 3, 4, 5, 6, 7)), c.a[i], acc[i][j], 0, 0, 0);
    PIN();
    if (LOAD) {
#pragma unroll
      for (int r = m * RPG; r < (m + 1) * RPG && r < NR; ++r) rd<BN, MT, AI>(l, r, ab, wb);
    }
    if (m == (NR + RPG - 1) / RPG) late();
    PIN();
  }
}
template <int BN, int MT, int AI, int AH, int STAGE, int NS, int MODE>
__device__ __forceinline__ void step(f32x4 (&acc)[4][BN / 32], Frag<BN / 32>& f0, Frag<BN / 32>& f1, u32& ab, u32& wb, int& st) {
  st = (st + 1 == NS) ? 0 : st + 1;
  const int dl = st == 0 ? -(NS - 1) * STAGE : STAGE;
  half<BN, MT, AI, true>(acc, f0, f1, ab + AH, wb + WL<BN>::KK, [&]() {
    ab += dl;
    wb += dl;
    asm volatile("" : "+v"(ab), "+v"(wb));
  });
  WAIT_LGKM0();
  if (!(MODE & 1)) BAR();
  PIN();
  half<BN, MT, AI, (MODE < 2)>(acc, f1, f0, ab, wb, [&]() {});
  if (MODE < 2) WAIT_LGKM0();
}

__device__ __forceinline__ u32 gelu2(f32x2 t, float c0, float c1, float sd) {
  const f32x2 z = t * fma2(t * t, dup2(c1), dup2(c0));
  const f32x2 e = {__builtin_amdgcn_exp2f(z[0]), __builtin_amdgcn_exp2f(z[1])};
  const f32x2 d = fma2(e, dup2(sd), dup2(sd));
  const f32x2 o = t * f32x2{__builtin_amdgcn_rcpf(d[0]), __builtin_amdgcn_rcpf(d[1])};
  return pack2(o[0], o[1]);
}
__device__ __forceinline__ u32x4 gelu16(f32x4 x, f32x4 y, f32x4 cs, f32x4 p, f32x4 q) {
  const u32 x01 = gelu2(fma2(lo2(cs), dup2(p[0]), lo2(x)), p[2], p[3], p[1]);
  const u32 x23 = gelu2(fma2(hi2(cs), dup2(p[0]), hi2(x)), p[2], p[3], p[1]);
  const u32 y01 = gelu2(fma2(lo2(cs), dup2(q[0]), lo2(y)), q[2], q[3], q[1]);
  const u32 y23 = gelu2(fma2(hi2(cs), dup2(q[0]), hi2(y)), q[2], q[3], q[1]);
  return swap16(x01, x23, y01, y23);
}
__device__ __forceinline__ f32x4 gelu_par(float nmean, float sd) {
  const float rs = __builtin_amdgcn_rcpf(sd);
  return f32x4{nmean, sd, -2.302208198f * rs, -0.1029432397f * rs * rs * rs};
}

struct Order {
  int cls, nxt;
  __device__ __forceinline__ Order(int c) : cls(c), nxt(c == 0 ? 1 : 0) {}
  __device__ __forceinline__ int operator()(int u) {
    if (u == 0) return cls;
    if (((u - 1) & 3) == cls && u + 3 < 16) return u + 3;
    const int kt = nxt;
    nxt += ((nxt + 1) & 3) == cls ? 2 : 1;
    return kt;
  }
};

struct Loader {
  rsrc_t rA, rW;
  u32 voA, voW, sa0, sa1, sw, la, lw, lb;
  int tab, n;
  __device__ __forceinline__ void W(int h, u32 k) {
    glds16<false>(rW, lb + lw + h * 8 * 1056, voW, sw + (k << 15));
    glds16<false>(rW, lb + lw + h * 8 * 1056 + 4 * 1056, voW, sw + (k << 15) + 8 * 2048);
  }
  template <bool BOTH>
  __device__ __forceinline__ void A(int h, u32 k) {
    glds16<true>(rA, lb + la + h * 8192, voA, sa0 + (k << 16));
    if (BOTH) glds16<true>(rA, lb + la + h * 8192 + 1024, voA, sa1 + (k << 16));
  }
  template <int M>
  __device__ __forceinline__ void issue() {
    const u32 k0 = __builtin_amdgcn_readlane(tab, n), k1 = __builtin_amdgcn_readlane(tab, n + 1);
    if (M & 2) A<!(M & 8)>(0, k0);
    if (M & 4) A<!(M & 8)>(1, k1);
    if (M & 1) {
      W(0, k0);
      W(1, k1);
    }
    n += 2;
    lb = (lb == 3 * ST2) ? 0 : lb + ST2;
  }
  __device__ __forceinline__ void seek(int stage) {
    n = 2 * stage;
    lb = (stage & 3) * ST2;
  }
};

__device__ __forceinline__ void load12(const Args& a, Ctx& c, int xcd, int idx) {
  const int lane = c.lane, w = c.w;
  const int tm = (xcd >> 1) * 4 + (idx >> 2), tn = (xcd & 1) * 4 + (idx & 3);
  const int m0 = tm * 128, n0 = tn * 128;
  const u32 voW = (4 * ((lane >> 4) & 1) + 16 * (lane >> 5)) * (D * 2) + (lane & 15) * 16;
  {
    const rsrc_t rA = mk_rsrc(a.x, S * D * 2), rW = mk_rsrc(a.w1, D * D * 2);
    const int sft = ((tn >> 2) + 1) * ((tn & 2) ? 1 : -1);
    u32 voA[4];
#pragma unroll
    for (int p = 0; p < 4; ++p) {
      const int r = m0 + sft + 32 * p + 8 * w + (lane >> 3);
      voA[p] = min(max(r, 0), S - 1) * (D * 2) + (((lane & 7) ^ (lane >> 4)) * 16);
    }
    Order ord(((idx >> 2) + idx) & 3);
    u32 lb = 0;
    auto issue = [&](int u) {
      const int kt = ord(u);
      const u32 sw = (kt * 64 + w) * (D * 2) + n0 * 2;
#pragma unroll
      for (int i = 0; i < 4; ++i) {
        glds16<false>(rW, lb + AB1 + (4 * i + w) * 1056, voW, sw + (8 * (i & 1) + 32 * (i >> 1)) * (D * 2));
        glds16<false>(rA, lb + (4 * i + w) * AP1, voA[i], kt * 128);
      }
      lb = (lb == 3 * ST1) ? 0 : lb + ST1;
    };
    issue(0);
    issue(1);
    WAIT_VM(8);
    BAR();
    issue(2);
#pragma unroll 1
    for (int k = 0; k <= 10; ++k) {
      issue(k + 3);
      WAIT_VM(16);
      BAR();
    }
    TAG_WAIT(c);
    const int rg = min(max(4 * tm - 1 + lane, 0), 63);
    const u32 fo = foff(F_S1 + (lane < 6 ? ((rg >> 4) * 2 + (xcd & 1)) * 16 + (rg & 15) : xcd * 16 + (tn & 3) * 4 + (lane & 3)), 0);
    issue(14);
    const u32x2 fv = peek(c, fo);
    WAIT_VM(17);
    BAR();
    issue(15);
    WAIT_VM(17);
    BAR();
    check<8>(a, c, fv, fo);
    glds16<true>(c.ws, L_TB + w * 1024, lane * 16,
                 w < 3 ? WS_XS + ((xcd & 1) * (S + 256) + 64 + m0 - 2 + 64 * w) * 16 : WS_C1 + ((xcd >> 1) * D + n0) * 4);
    const rsrc_t r2 = mk_rsrc(a.w2, D * D * 2);
#pragma unroll
    for (int i = 0; i < 4; ++i)
      touch(r2, L_DUM + w * 256, (lane >> 4) * 2048 + (lane & 15) * 64, (idx * 64 + w * 16 + i * 4) * 2048 + (xcd & 1) * 1024);
    BAR();
    WAIT_VM(5);
    BAR();
    WAIT_VM(4);
    BAR();
  }
  STAMP_DECL; STAMP(0);
  Loader L;
  L.rA = c.ws;
  L.rW = mk_rsrc(a.w2, D * D * 2);
  L.voA = lane * 16;
  L.voW = voW;
  L.sa0 = WS_H + (w & 1) * 65536 + (m0 + (w >> 1) * 64) * 32;
  L.sa1 = L.sa0 + 1024;
  L.sw = w * (D * 2) + n0 * 2;
  L.la = (w & 1) * 4096 + (w >> 1) * 2048;
  L.lw = AB + w * 1056;
  {
    const int m = lane < 18 ? lane - 4 : lane - 18;
    L.tab = lane < 4 ? tn * 8 + 2 * lane : ((tn + 1 + (m >> 1)) & 7) * 8 + 4 * (m & 1) + (lane < 18 ? 0 : 2);
  }
  L.seek(0);
#pragma unroll
  for (int s = 0; s < 4; ++s) L.issue<1>();
  WAIT_VM(0);
  arrive(c, 2);
  STAMP(1);
  poll(a, c, foff(F_HE + tm * 8 + ((lane >> 2) & 7), lane & 3));
  STAMP(2);
  L.seek(2);
  L.issue<6>();
  L.issue<6>();
  WAIT_VM(4);
  STAMP(3);
  BAR();
  L.issue<7>();
  L.issue<7>();
  WAIT_VM(16);
  BAR();
  const u32 fo = foff(F_HL + tm * 8 + ((lane >> 2) & 7), lane & 3);
  L.issue<7>();
  const u32x2 fv = peek(c, fo);
  WAIT_VM(17);
  BAR();
  L.issue<7>();
  WAIT_VM(17);
  BAR();
  L.issue<7>();
  check<16>(a, c, fv, fo);
  BAR();
#pragma unroll 1
  for (int k = 6; k <= 12; ++k) {
    L.issue<7>();
    WAIT_VM(16);
    BAR();
  }
  WAIT_VM(8);
  BAR();
  WAIT_VM(0);
  BAR();
  STAMP_OUT(a.ws, 7);
  BAR();
}

__device__ __forceinline__ void cons12(const Args& a, Ctx& c, int xcd, int idx) {
  const int lane = c.lane, w = c.w;
  const int tm = (xcd >> 1) * 4 + (idx >> 2), tn = (xcd & 1) * 4 + (idx & 3);
  const int m0 = tm * 128, n0 = tn * 128;
  const int wm = w >> 1, wn = w & 1, r16 = lane & 15, g = lane >> 4;
  const u32 wb0 = wn * 128 + 8 * (lane & 3) + ((r16 >> 2) + 4 * (g & 1)) * 1056 + (g >> 1) * 512;
  const u32 vo = (16 * (g & 1) + r16) * 32 + (g >> 1) * 16;
  f32x4 acc[4][4];
  Frag<4> f0, f1;
  u32x4 xr[4][2];
  {
    STAMP_DECL; STAMP(0);
    if (w == 0 && lane < 4) LDS(u32, L_CNT + 4 * lane) = 0u;
    u32 ab = (wm * 8 + (r16 >> 3)) * AP1 + (r16 & 7) * 128 + ((g ^ ((r16 >> 1) & 3)) * 16), wb = AB1 + wb0;
#pragma unroll
    for (int i = 0; i < 4; ++i)
#pragma unroll
      for (int j = 0; j < 4; ++j) {
        acc[i][j] = f32x4{0.f, 0.f, 0.f, 0.f};
        asm volatile("" : "+v"(acc[i][j]));
      }
    TAG_WAIT(c);
    BAR();
    STAMP(1);
#pragma unroll
    for (int r = 0; r < 12; ++r) rd<128, 4, 2 * AP1>(f0, r, ab, wb);
    WAIT_LGKM0();
    PIN();
    int st = 0;
#pragma unroll 1
    for (int kt = 0; kt < 15; ++kt) step<128, 4, 2 * AP1, 64, ST1, 4, 0>(acc, f0, f1, ab, wb, st);
#pragma unroll
    for (int i = 0; i < 4; ++i)
#pragma unroll
      for (int j = 0; j < 2; ++j)
        xr[i][j] = *(const u32x4*)(a.x + (size_t)(m0 + wm * 64 + 16 * i + r16) * D + n0 + wn * 64 + 32 * j + 16 * (g & 1) + 8 * (g >> 1));
    PIN();
    step<128, 4, 2 * AP1, 64, ST1, 4, 2>(acc, f0, f1, ab, wb, st);
    STAMP(2);
    const int sft = ((tn >> 2) + 1) * ((tn & 2) ? 1 : -1);
    f32x4 p[4];
#pragma unroll
    for (int i = 0; i < 4; ++i) {
      f32x2 ms = LDS(f32x2, L_TB + (2 + sft + wm * 64 + 16 * i + r16) * 16 + 8);
      if ((i == 0 || i == 3) && (tm == 0 || tm == 15)) {
        const bool in = (u32)(m0 + sft + wm * 64 + 16 * i + r16) < (u32)S;
        ms = in ? ms : f32x2{0.f, 1.f};
#pragma unroll
        for (int j = 0; j < 4; ++j) acc[i][j] = in ? acc[i][j] : f32x4{0.f, 0.f, 0.f, 0.f};
      }
      p[i] = gelu_par(ms[0], ms[1]);
    }
#pragma unroll
    for (int j = 0; j < 4; ++j) {
      const f32x4 cs = LDS(f32x4, L_TB + 3072 + (wn * 64 + 16 * j + 4 * g) * 4);
#pragma unroll
      for (int ip = 0; ip < 2; ++ip) {
        const u32x4 v = gelu16(acc[2 * ip][j], acc[2 * ip + 1][j], cs, p[2 * ip], p[2 * ip + 1]);
        st16(c.ws, vo, WS_H + (tn * 8 + wn * 4 + j) * 65536 + (m0 + wm * 64 + ip * 32) * 32, v);
        LDS(u32x4, wn * ST2 + j * 4096 + (wm * 64 + ip * 32) * 32 + vo) = v;
      }
      if (j == 2) {
        WAIT_VM(2);
        set_flag(c, F_HE + tm * 8 + tn, w);
      }
    }
    STAMP(3);
    STAMP_OUT(a.ws, 1);
  }
  STAMP_DECL; STAMP(0);
  asm volatile("" ::: "memory");
  arrive(c, 2);
#pragma unroll
  for (int i = 0; i < 4; ++i) {
    const f32x2 xs = LDS(f32x2, L_TB + (2 + wm * 64 + 16 * i + r16) * 16);
#pragma unroll
    for (int j = 0; j < 2; ++j) {
      const u32x4 xv = swap16(xr[i][j][0], xr[i][j][1], xr[i][j][2], xr[i][j][3]);
#pragma unroll
      for (int h = 0; h < 2; ++h) {
        const f32x2 l = fma2(bf_2(xv[2 * h]), dup2(xs[0]), dup2(xs[1])), u = fma2(bf_2(xv[2 * h + 1]), dup2(xs[0]), dup2(xs[1]));
        acc[i][2 * j + h] = f32x4{l[0], l[1], u[0], u[1]};
        asm volatile("" : "+v"(acc[i][2 * j + h]));
      }
    }
  }
  u32 ab = (g >> 1) * 4096 + (wm * 64 + r16) * 32 + (g & 1) * 16, wb = AB + wb0;
  soft_wait(2);
  STAMP(1);
#pragma unroll
  for (int r = 0; r < 12; ++r) rd<128, 4, 512>(f0, r, ab, wb);
  WAIT_LGKM0();
  PIN();
  int st = 0;
  step<128, 4, 512, 8192, ST2, 4, 1>(acc, f0, f1, ab, wb, st);
  WAIT_VM(0);
  set_flag(c, F_HL + tm * 8 + tn, w);
  PIN();
#pragma unroll 1
  for (int kt = 1; kt < 15; ++kt) step<128, 4, 512, 8192, ST2, 4, 0>(acc, f0, f1, ab, wb, st);
  step<128, 4, 512, 8192, ST2, 4, 2>(acc, f0, f1, ab, wb, st);
  STAMP(2);
#pragma unroll
  for (int j = 0; j < 4; ++j)
#pragma unroll
    for (int ip = 0; ip < 2; ++ip) {
      const f32x4 x = acc[2 * ip][j], y = acc[2 * ip + 1][j];
      st16(c.ws, vo, WS_X1 + (tn * 8 + wn * 4 + j) * 65536 + (m0 + wm * 64 + ip * 32) * 32,
           swap16(pack2(x[0], x[1]), pack2(x[2], x[3]), pack2(y[0], y[1]), pack2(y[2], y[3])));
    }
  PIN();
  float t1[4], t2[4];
#pragma unroll
  for (int i = 0; i < 4; ++i) {
    f32x2 s1 = lo2(acc[i][0]) + hi2(acc[i][0]), s2 = fma2(lo2(acc[i][0]), lo2(acc[i][0]), hi2(acc[i][0]) * hi2(acc[i][0]));
#pragma unroll
    for (int j = 1; j < 4; ++j) {
      s1 += lo2(acc[i][j]) + hi2(acc[i][j]);
      s2 = fma2(lo2(acc[i][j]), lo2(acc[i][j]), fma2(hi2(acc[i][j]), hi2(acc[i][j]), s2));
    }
    t1[i] = s1[0] + s1[1];
    t2[i] = s2[0] + s2[1];
  }
  const float r1 = red16(red32(t1[0], t1[2]), red32(t1[1], t1[3])), r2 = red16(red32(t2[0], t2[2]), red32(t2[1], t2[3]));
  __builtin_amdgcn_raw_buffer_store_b64(u32x2{__float_as_uint(r1), __float_as_uint(r2)}, c.ws, lane * 8,
                                        WS_ST + ((tm * 16 + tn * 2 + wn) * 128 + wm * 64) * 8, 16);
  WAIT_VM(1);
  set_flag(c, F_X1 + tm * 8 + tn, w);
  STAMP(3);
  WAIT_VM(1);
  set_flag(c, F_ST + tm * 8 + tn, w);
  STAMP_OUT(a.ws, 2);
}

__device__ __forceinline__ void load34(const Args& a, const Ctx& c, int xcd, int idx) {
  const int lane = fresh(c.lane), w = c.w;
  const int tm = (xcd >> 2) * 8 + (idx >> 2), m0 = tm * 128, cls = ((idx >> 2) + idx) & 3;
  {
    const int n0 = ((xcd & 3) * 4 + (idx & 3)) * 224;
    const rsrc_t rW = mk_rsrc(a.w3, D * F * 2);
    u32 voW[7];
#pragma unroll
    for (int i = 0; i < 7; ++i) {
      const int l = i * 64 + lane, r = l / 28, q = l % 28, wn = q / 14, cc = q % 14;
      voW[i] = ((r & 7) + 16 * (r >> 3)) * (F * 2) + (cc < 12 ? wn * 12 + cc : 12 + 2 * wn + cc) * 16;
    }
    const u32 sa = WS_X1 + w * 65536 + m0 * 32, sw = (8 * (w & 1) + 32 * (w >> 1)) * (F * 2) + n0 * 2;
    u32 lb = 0;
    auto isW = [&](int kt) {
#pragma unroll
      for (int i = 0; i < 7; ++i) glds16<false>(rW, lb + AB + w * 7200 + i * 1024, voW[i], sw + kt * (64 * F * 2));
    };
    auto isA = [&](int kt) {
#pragma unroll
      for (int i = 0; i < 4; ++i) glds16<true>(c.ws, lb + w * 4096 + i * 1024, lane * 16, sa + kt * 262144 + i * 1024);
    };
    auto adv = [&]() { lb = (lb == 2 * ST3) ? 0 : lb + ST3; };
    Order ord(cls);
    const int k0 = ord(0), k1 = ord(1), k2 = ord(2);
    isW(k0); adv();
    isW(k1); adv();
    isW(k2); adv();
    if (idx >= 16) {
      WAIT_VM(21);
      if (arrive(c, 1) == 7) set_flag(c, F_C + xcd * 16 + idx - 16, 0);
    }
    poll(a, c, foff(F_X1 + tm * 8 + ((lane >> 2) & 7), lane & 3));
    isA(k0); adv();
    isA(k1); adv();
    isA(k2); adv();
    WAIT_VM(8);
    BAR();
    WAIT_VM(4);
    BAR();
    auto issue = [&](int u) {
      const int kt = ord(u);
      isA(kt);
      isW(kt);
      adv();
    };
#pragma unroll 1
    for (int k = 1; k <= 7; ++k) {
      issue(k + 2);
      WAIT_VM(11);
      BAR();
    }
    const u32 fo0 = foff(F_ST + tm * 8 + ((lane >> 2) & 7), lane & 3), fo1 = foff(F_C + lane, 0), fo2 = foff(F_C + 64 + lane, 0);
    issue(10);
    const u32x2 fv0 = peek(c, fo0), fv1 = peek(c, fo1), fv2 = peek(c, fo2);
    WAIT_VM(14);
    BAR();
    issue(11);
    check<11>(a, c, fv0, fo0);
    check<11>(a, c, fv1, fo1);
    check<11>(a, c, fv2, fo2);
    BAR();
    issue(12);
    u32x4 sv[4];
    u32 cv[4];
    const int tl = min(w * 64 + lane, 223), q = tl >> 3, cc = q % 14;
#pragma unroll
    for (int i = 0; i < 4; ++i) {
      sv[i] = ld16(c.ws, (lane >> 4) * 4096 + (lane & 15) * 16, WS_ST + (tm * 16 + i) * 1024 + w * 256);
      cv[i] = __builtin_amdgcn_raw_buffer_load_b32(c.ws, ((cc < 12 ? (q / 14) * 12 + cc : 12 + 2 * (q / 14) + cc) * 8 + (tl & 7)) * 4,
                                                   WS_C3 + (i * F + n0) * 4, 16);
    }
    WAIT_VM(19);
    BAR();
#pragma unroll 1
    for (int k = 11; k <= 13; ++k) {
      issue(k + 2);
      WAIT_VM(11);
      BAR();
    }
    WAIT_VM(0);
    BAR();
    PIN();
    const f32x4 t = (__builtin_bit_cast(f32x4, sv[0]) + __builtin_bit_cast(f32x4, sv[1])) + (__builtin_bit_cast(f32x4, sv[2]) + __builtin_bit_cast(f32x4, sv[3]));
    float s1 = red32(t[0], t[2]), s2 = red32(t[1], t[3]);
    s1 = red16(s1, s1);
    s2 = red16(s2, s2);
    const float mean = s1 * (1.0f / D);
    const float var = fmaxf(s2 * (1.0f / D) - mean * mean, 0.f);
    LDS(f32x4, L_T3 + (32 * w + 2 * (lane & 15) + (lane >> 5)) * 16) = gelu_par(-rbf(mean), rbf(__builtin_amdgcn_sqrtf(rbf(rbf(var) + 1e-5f))));
    LDS(float, L_T3 + 2048 + tl * 4) = (__uint_as_float(cv[0]) + __uint_as_float(cv[1])) + (__uint_as_float(cv[2]) + __uint_as_float(cv[3]));
    WAIT_LGKM0();
    BAR();
  }
  const int blk = (xcd & 1) * 4 + (idx & 3), p = (xcd >> 1) & 1, n0 = blk * 128;
  Loader L;
  L.rA = c.ws;
  L.rW = mk_rsrc(a.w4, F * D * 2);
  L.voA = lane * 16;
  L.voW = (4 * ((lane >> 4) & 1) + 16 * (lane >> 5)) * (D * 2) + (lane & 15) * 16;
  L.sa0 = WS_G3 + (w & 1) * 65536 + (m0 + p * 64 + (w >> 1) * 32) * 32;
  L.sa1 = L.sa0 ^ 2048;
  L.sw = w * (D * 2) + n0 * 2;
  L.la = (w & 1) * 4096 + (w >> 1) * 2048;
  L.lw = AB + w * 1056;
  {
    const int e = lane < 2 * NFS ? lane : 2 * NFS + ((lane - 2 * NFS) >> 1), pp = (lane >= 2 * NFS && (lane & 1)) ? p ^ 1 : p;
    const int m = e - 21, q = m / 5, r = (m + cls) % 5;
    const int k = e < 7 ? blk * 7 + e : e < 21 ? ((blk + 1 + ((e - 7) >> 1)) & 7) * 7 + 3 * ((e - 7) & 1) : (q + (q >= blk)) * 7 + r + 1 + (r >= 2);
    L.tab = pp * 112 + 2 * min(k, 55);
  }
  L.seek(0);
#pragma unroll
  for (int s = 0; s < 4; ++s) L.issue<1>();
#pragma unroll
  for (int i = 0; i < 4; ++i)
    glds16<true>(c.ws, L_TB + w * 4096 + i * 1024, (lane >> 3) * (S * 2) + (((lane & 7) ^ (lane >> 3 & 6)) * 16),
                 WS_YT + (n0 + w * 32 + 8 * i) * (S * 2) + (m0 + p * 64) * 2);
  WAIT_VM(4);
  arrive(c, 3);
  poll(a, c, foff(F_GE + tm * 16 + p * 8 + ((lane >> 2) & 7), lane & 3));
  L.seek(3);
  L.issue<4>();
  BAR();
  L.issue<7>();
  BAR();
  L.issue<7>();
  WAIT_VM(16);
  BAR();
  L.issue<7>();
  WAIT_VM(16);
  BAR();
  const u32 fo = foff(F_GL + tm * 16 + (lane >> 2), lane & 3);
  L.issue<7>();
  const u32x2 fv = peek(c, fo);
  WAIT_VM(17);
  BAR();
  L.issue<7>();
  WAIT_VM(17);
  BAR();
  L.issue<7>();
  check<16>(a, c, fv, fo);
  BAR();
#pragma unroll 1
  for (int k = 7; k <= NFS - 4; ++k) {
    L.issue<7>();
    WAIT_VM(16);
    BAR();
  }
  L.issue<15>();
  WAIT_VM(14);
  BAR();
#pragma unroll 1
  for (int k = 1; k < TAIL; ++k) {
    L.issue<15>();
    WAIT_VM(12);
    BAR();
  }
  WAIT_VM(6);
  BAR();
  WAIT_VM(0);
  BAR();
  if (blockIdx.x == 0 && w == 0) {
#pragma unroll 1
    for (int i = 0; i < 4; ++i) poll(a, c, foff(F_GL + i * 64 + lane, 0));
    if (lane == 0) __hip_atomic_store((gu64*)a.ws, c.tag, __ATOMIC_RELAXED, __HIP_MEMORY_SCOPE_AGENT);
  }
}

#define TA 3
#define TB 2
__device__ __forceinline__ void cons34(const Args& a, const Ctx& c, int xcd, int idx) {
  const int lane = fresh(c.lane), w = c.w;
  const int tm = (xcd >> 2) * 8 + (idx >> 2), m0 = tm * 128;
  const int wm = w >> 1, wn = w & 1, r16 = lane & 15, g = lane >> 4;
  const int blk = (xcd & 1) * 4 + (idx & 3), p = (xcd >> 1) & 1, n0 = blk * 128;
  const int row0 = m0 + p * 64 + wm * 32;
  const u32 vo = (16 * (g & 1) + r16) * 32 + (g >> 1) * 16;
  const u32 ab0 = (g >> 1) * 4096 + (wm * 64 + r16) * 32 + (g & 1) * 16;
  u32x4 xr[2][2], x1r[4];
  f32x2 xs[2];
  {
    STAMP_DECL; STAMP(0);
    const int tn = (xcd & 3) * 4 + (idx & 3);
    f32x4 acc[4][7];
    Frag<7> f0, f1;
    u32 ab = ab0, wb = AB + wn * 224 + 8 * (lane & 3) + (g & 1) * 7200 + ((r16 >> 2) + 8 * (g >> 1)) * 448;
#pragma unroll
    for (int i = 0; i < 4; ++i)
#pragma unroll
      for (int j = 0; j < 7; ++j) {
        acc[i][j] = f32x4{0.f, 0.f, 0.f, 0.f};
        asm volatile("" : "+v"(acc[i][j]));
      }
    if (idx >= 16) {
      WAIT_VM(0);
      if (arrive(c, 1) == 7) set_flag(c, F_C + xcd * 16 + idx - 16, 0);
    }
    BAR();
    STAMP(1);
#pragma unroll
    for (int r = 0; r < 18; ++r) rd<224, 4, 512>(f0, r, ab, wb);
    WAIT_LGKM0();
    PIN();
    int st = 0;
#pragma unroll 1
    for (int kt = 0; kt < 15; ++kt) step<224, 4, 512, 8192, ST3, 3, 0>(acc, f0, f1, ab, wb, st);
    step<224, 4, 512, 8192, ST3, 3, 2>(acc, f0, f1, ab, wb, st);
    STAMP(2);
#pragma unroll
    for (int j = 0; j < 4; ++j) x1r[j] = ld16(c.ws, vo, WS_X1 + (blk * 8 + wn * 4 + j) * 65536 + row0 * 32);
#pragma unroll
    for (int i = 0; i < 2; ++i) {
      xs[i] = __builtin_bit_cast(f32x2, __builtin_amdgcn_raw_buffer_load_b64(c.ws, r16 * 16, WS_XS + (64 + row0 + 16 * i) * 16, 16));
#pragma unroll
      for (int j = 0; j < 2; ++j)
        xr[i][j] = *(const u32x4*)(a.x + (size_t)(row0 + 16 * i + r16) * D + n0 + wn * 64 + 32 * j + 16 * (g & 1) + 8 * (g >> 1));
    }
    PIN();
    f32x4 q[4];
#pragma unroll
    for (int i = 0; i < 4; ++i) q[i] = LDS(f32x4, L_T3 + (wm * 64 + 16 * i + r16) * 16);
#pragma unroll
    for (int j = 0; j < 7; ++j) {
      const f32x4 cs = LDS(f32x4, L_T3 + 2048 + (wn * 112 + 16 * j + 4 * g) * 4);
      const int kg = j < 6 ? wn * 6 + j : 12 + wn;
#pragma unroll
      for (int ip = 0; ip < 2; ++ip) {
        const u32x4 v = gelu16(acc[2 * ip][j], acc[2 * ip + 1][j], cs, q[2 * ip], q[2 * ip + 1]);
        st16(c.ws, vo, WS_G3 + (tn * 14 + kg) * 65536 + (m0 + wm * 64 + ip * 32) * 32, v);
        LDS(u32x4, (kg >> 2) * ST2 + (kg & 3) * 4096 + (2 * ip + (wm ^ p)) * 1024 + vo) = v;
      }
      if (j == 3) {
        WAIT_VM(4);
        set_flag(c, F_GE + tm * 16 + tn, w);
      }
    }
    STAMP(3);
    STAMP_OUT(a.ws, 4);
  }
  STAMP_DECL; STAMP(0);
  asm volatile("" ::: "memory");
  arrive(c, 3);
  f32x4 acc[4][4];
  Frag<4> f0, f1;
#pragma unroll
  for (int j = 0; j < 4; ++j) {
    const u32x4 xv = swap16(x1r[j][0], x1r[j][1], x1r[j][2], x1r[j][3]);
#pragma unroll
    for (int i = 0; i < 4; ++i) {
      acc[i][j] = i < 2 ? f32x4{bf_lo(xv[2 * i]), bf_hi(xv[2 * i]), bf_lo(xv[2 * i + 1]), bf_hi(xv[2 * i + 1])} : f32x4{0.f, 0.f, 0.f, 0.f};
      asm volatile("" : "+v"(acc[i][j]));
    }
  }
  u32 ab = ab0, wb = AB + wn * 128 + 8 * (lane & 3) + ((r16 >> 2) + 4 * (g & 1)) * 1056 + (g >> 1) * 512;
  soft_wait(3);
  STAMP(1);
#pragma unroll
  for (int r = 0; r < 12; ++r) rd<128, 4, 512>(f0, r, ab, wb);
  WAIT_LGKM0();
  PIN();
  int st = 0;
#pragma unroll 1
  for (int kt = 0; kt < NFS; ++kt) {
    step<128, 4, 512, 8192, ST2, 4, 0>(acc, f0, f1, ab, wb, st);
    if (kt == 1) {
      WAIT_VM(0);
      set_flag(c, F_GL + tm * 16 + (xcd & 3) * 4 + (idx & 3), w);
    }
  }
#pragma unroll
  for (int i = 0; i < 8; ++i) st16(c.ws, lane * 16, WS_EX + (blockIdx.x * 4 + w) * 8192 + i * 1024, __builtin_bit_cast(u32x4, acc[2 + (i >> 2)][i & 3]));
  PIN();
  f32x4 sv[2][4];
  u32x4 xv[2][2];
#pragma unroll
  for (int i = 0; i < 2; ++i)
#pragma unroll
    for (int j = 0; j < 4; ++j) {
      if (!(j & 1)) xv[i][j / 2] = swap16(xr[i][j / 2][0], xr[i][j / 2][1], xr[i][j / 2][2], xr[i][j / 2][3]);
      const int dr = wn * 64 + 16 * j + 4 * g + (r16 >> 2);
      const u32x2 yv = __builtin_bit_cast(u32x2, lds_tr(L_TB + dr * 128 + (((2 * wm + i) ^ (dr >> 1 & 3)) * 32) + 8 * (lane & 3)));
      const f32x2 l = fma2(bf_2(xv[i][j / 2][2 * (j & 1)]), dup2(xs[i][0]), dup2(xs[i][1])) * bf_2(yv[0]);
      const f32x2 u = fma2(bf_2(xv[i][j / 2][2 * (j & 1) + 1]), dup2(xs[i][0]), dup2(xs[i][1])) * bf_2(yv[1]);
      sv[i][j] = f32x4{l[0], l[1], u[0], u[1]};
      asm volatile("" : "+v"(sv[i][j]));
    }
  PIN();
#pragma unroll 1
  for (int kt = 0; kt < TA; ++kt) step<128, 2, 512, 8192, ST2, 4, 0>(acc, f0, f1, ab, wb, st);
  WAIT_VM(0);
  set_flag(c, F_EX + blockIdx.x, w);
  PIN();
#pragma unroll 1
  for (int kt = 0; kt < TB; ++kt) step<128, 2, 512, 8192, ST2, 4, 0>(acc, f0, f1, ab, wb, st);
  poll(a, c, foff(F_EX + (blockIdx.x ^ 2), w));
  u32x4 ex[8];
#pragma unroll
  for (int i = 0; i < 8; ++i) ex[i] = ld16(c.ws, lane * 16, WS_EX + ((blockIdx.x ^ 2) * 4 + w) * 8192 + i * 1024);
  PIN();
#pragma unroll 1
  for (int kt = 0; kt < TAIL - TA - TB - 1; ++kt) step<128, 2, 512, 8192, ST2, 4, 0>(acc, f0, f1, ab, wb, st);
  step<128, 2, 512, 8192, ST2, 4, 3>(acc, f0, f1, ab, wb, st);
  STAMP(2);
#pragma unroll
  for (int i = 0; i < 2; ++i)
#pragma unroll
    for (int jp = 0; jp < 2; ++jp) {
      u32 o[4];
#pragma unroll
      for (int e = 0; e < 4; ++e) {
        const int j = 2 * jp + (e >> 1);
        const f32x4 t = acc[i][j] + __builtin_bit_cast(f32x4, ex[4 * i + j]);
        const f32x2 ob = (e & 1) ? hi2(t) : lo2(t);
        const f32x2 z = ob * dup2(-1.442695041f);
        const f32x2 d = f32x2{__builtin_amdgcn_exp2f(z[0]), __builtin_amdgcn_exp2f(z[1])} + dup2(1.0f);
        const f32x2 gt = ob * f32x2{__builtin_amdgcn_rcpf(d[0]), __builtin_amdgcn_rcpf(d[1])};
        const f32x2 r = fma2((e & 1) ? hi2(sv[i][j]) : lo2(sv[i][j]), gt, bf_2(xv[i][jp][e]));
        o[e] = pack2(r[0], r[1]);
      }
      *(u32x4*)(a.out + (size_t)(row0 + 16 * i + r16) * D + n0 + wn * 64 + 32 * jp + 16 * (g & 1) + 8 * (g >> 1)) = swap16(o[0], o[1], o[2], o[3]);
    }
  STAMP(3);
  STAMP_OUT(a.ws, 5);
}

__global__ __launch_bounds__(512, 2) void fused(Args a) {
  const int xcd = blockIdx.x & 7, idx = blockIdx.x >> 3;
  const int w8 = __builtin_amdgcn_readfirstlane(threadIdx.x >> 6);
  Ctx c;
  c.tag = *(const __attribute__((address_space(4))) u64*)a.ws;
  c.ws = mk_rsrc(a.ws, WS_END);
  c.lane = threadIdx.x & 63;
  c.w = w8 & 3;
  if (idx < 16) {
    if (w8 >= 4) load12(a, c, xcd, idx);
    else cons12(a, c, xcd, idx);
  } else {
    if (w8 >= 4) side_kv(a, c, xcd, idx - 16);
    else side_stats(a, c, xcd, idx - 16);
    conv(a, c, w8, xcd, idx - 16);
  }
  if (w8 >= 4) load34(a, c, xcd, idx);
  else cons34(a, c, xcd, idx);
}

extern "C" void kernel_launch(void* const* d_in, const int* in_sizes, int n_in, void* d_out, int out_size, void* d_ws,
                              size_t ws_size, hipStream_t stream) {
  Args a;
  a.x = (const u16*)d_in[0];
  a.k = (const u16*)d_in[1];
  a.v = (const u16*)d_in[2];
  a.shk = (const u16*)d_in[3];
  a.ssm_k = (const u16*)d_in[4];
  a.ssm_D = (const u16*)d_in[5];
  a.w1 = (const u16*)d_in[7];
  a.w2 = (const u16*)d_in[8];
  a.w3 = (const u16*)d_in[9];
  a.w4 = (const u16*)d_in[10];
  a.ws = (char*)d_ws;
  a.out = (u16*)d_out;
  (void)hipFuncSetAttribute((const void*)fused, hipFuncAttributeMaxDynamicSharedMemorySize, LDS_BYTES);
  fused<<<256, 512, LDS_BYTES, stream>>>(a);
}
```

```cpp
#include <hip/hip_runtime.h>
#include <hip/hip_bf16.h>
#include <stdint.h>
#pragma clang diagnostic ignored "-Wint-to-pointer-cast"

typedef unsigned short u16;
typedef unsigned int u32;
typedef unsigned long long u64;
typedef __attribute__((ext_vector_type(8))) __bf16 bf16x8;
typedef __attribute__((ext_vector_type(4))) short s16x4;
typedef __attribute__((ext_vector_type(4))) float f32x4;
typedef __attribute__((ext_vector_type(2))) float f32x2;
typedef __attribute__((ext_vector_type(2))) u32 u32x2;
typedef __attribute__((ext_vector_type(4))) u32 u32x4;
typedef __attribute__((ext_vector_type(2))) __bf16 bf16x2;
typedef __attribute__((address_space(1))) u64 gu64;
typedef __amdgpu_buffer_rsrc_t rsrc_t;
#define AS3 __attribute__((address_space(3)))
#define LDS(T, off) (*(AS3 T*)(u32)(off))

constexpr int S = 2048, D = 1024, F = 3584;

constexpr u32 WS_FL = 1024;
constexpr u32 F_KV = 0, F_C = 128, F_HE = 256, F_HL = 384, F_X1 = 512, F_ST = 640, F_GE = 768, F_GL = 1024, F_EX = 1280;
constexpr u32 WS_XS = WS_FL + 1536 * 128;
constexpr u32 WS_C1 = WS_XS + S * 16;
constexpr u32 WS_C3 = WS_C1 + 32 * 1024;
constexpr u32 WS_ST = WS_C3 + 4 * F * 4;
constexpr u32 WS_H = 1u << 20;
constexpr u32 WS_X1 = WS_H + S * D * 2;
constexpr u32 WS_G3 = WS_X1 + S * D * 2;
constexpr u32 WS_UR = WS_G3 + S * F * 2;
constexpr u32 WS_YT = WS_UR + D * S * 2;
constexpr u32 WS_EX = WS_YT + D * S * 2;
constexpr u32 WS_END = WS_EX + 256 * 32768;
static_assert(WS_ST + 16 * 16 * 128 * 8 <= WS_H, "ws");

constexpr int CV_H = 4576, CV_U = 4192, CV_CH = 2 * CV_H + 2 * CV_U;
constexpr u32 L_TB = 8 * CV_CH;
constexpr u32 L_T3 = L_TB + 16384;
constexpr u32 L_DUM = L_T3 + 3072;
constexpr u32 L_CNT = L_DUM + 2048;
enum { E_SUM, E_KV, E_CC, E_CL, E_2C, E_2L, E_HE, E_HGO, E_2S, E_X1, E_XGO, E_4C, E_4L, E_GE, E_GGO, E_4S0, E_4S1, E_N };
constexpr int LDS_BYTES = 163840;
static_assert(L_CNT + 16 * E_N <= LDS_BYTES && E_N <= 64, "lds");
constexpr u32 OOB = 0x80000000u;

__device__ __forceinline__ float bf_lo(u32 v) { return __uint_as_float(v << 16); }
__device__ __forceinline__ float bf_hi(u32 v) { return __uint_as_float(v & 0xffff0000u); }
__device__ __forceinline__ f32x2 bf_2(u32 v) { return f32x2{bf_lo(v), bf_hi(v)}; }
__device__ __forceinline__ u32 pack2(float lo, float hi) {
  bf16x2 r;
  r[0] = (__bf16)lo;
  r[1] = (__bf16)hi;
  return __builtin_bit_cast(u32, r);
}
__device__ __forceinline__ float rbf(float x) { return (float)(__bf16)x; }
__device__ __forceinline__ f32x2 fma2(f32x2 a, f32x2 b, f32x2 c) { return __builtin_elementwise_fma(a, b, c); }
__device__ __forceinline__ f32x2 dup2(float a) { return f32x2{a, a}; }
__device__ __forceinline__ f32x2 lo2(f32x4 v) { return f32x2{v[0], v[1]}; }
__device__ __forceinline__ f32x2 hi2(f32x4 v) { return f32x2{v[2], v[3]}; }
__device__ __forceinline__ float dot2(u32 a, u32 b, float c) {
  return __builtin_amdgcn_fdot2_f32_bf16(__builtin_bit_cast(bf16x2, a), __builtin_bit_cast(bf16x2, b), c, false);
}
__device__ __forceinline__ u32 sconst(u32 v) {
  asm("" : "+s"(v));
  return v;
}
template <int CTRL>
__device__ __forceinline__ float dpp_add(float v) {
  return v + __int_as_float(__builtin_amdgcn_update_dpp(0, __float_as_int(v), CTRL, 0xf, 0xf, true));
}
__device__ __forceinline__ float red32(float a, float b) {
  const u32x2 s = __builtin_amdgcn_permlane32_swap(__float_as_uint(a), __float_as_uint(b), false, false);
  return __uint_as_float(s[0]) + __uint_as_float(s[1]);
}
__device__ __forceinline__ float red16(float a, float b) {
  const u32x2 s = __builtin_amdgcn_permlane16_swap(__float_as_uint(a), __float_as_uint(b), false, false);
  return __uint_as_float(s[0]) + __uint_as_float(s[1]);
}
__device__ __forceinline__ u32x4 swap16(u32 x01, u32 x23, u32 y01, u32 y23) {
  const u32x2 s0 = __builtin_amdgcn_permlane16_swap(x01, y01, false, false);
  const u32x2 s1 = __builtin_amdgcn_permlane16_swap(x23, y23, false, false);
  return u32x4{s0[0], s1[0], s0[1], s1[1]};
}

#define STAMP(i)
#define STAMP_DECL
#define STAMP_OUT(ws, kid)
#define WAIT_VM(n) asm volatile("s_waitcnt vmcnt(%0)" ::"n"(n) : "memory")
#define WAIT_LGKM0() __builtin_amdgcn_s_waitcnt(0xC07F)
#define PIN() __builtin_amdgcn_sched_barrier(0)
#define BAR() __builtin_amdgcn_s_barrier()
#define TR_DECL
#define TRA()
#define LWAIT(n) WAIT_VM(n)
#define LBAR() BAR()
#define TR_OUT(ws, ph)

__device__ __forceinline__ rsrc_t mk_rsrc(const void* p, u32 bytes) {
  return __builtin_amdgcn_make_buffer_rsrc((void*)p, 0, bytes, 0x00020000);
}
template <bool SC1>
__device__ __forceinline__ void glds16(rsrc_t r, u32 lds_off, u32 voff, u32 soff) {
  if (SC1)
    asm volatile("s_mov_b32 m0, %0\n\ts_nop 0\n\tbuffer_load_dwordx4 %1, %2, %3 offen sc1 lds" ::"s"(lds_off), "v"(voff), "s"(r), "s"(soff) : "memory");
  else
    asm volatile("s_mov_b32 m0, %0\n\ts_nop 0\n\tbuffer_load_dwordx4 %1, %2, %3 offen lds" ::"s"(lds_off), "v"(voff), "s"(r), "s"(soff) : "memory");
}
__device__ __forceinline__ void touch(rsrc_t r, u32 lds_off, u32 voff, u32 soff) {
  asm volatile("s_mov_b32 m0, %0\n\ts_nop 0\n\tbuffer_load_dword %1, %2, %3 offen lds" ::"s"(lds_off), "v"(voff), "s"(r), "s"(soff) : "memory");
}
__device__ __forceinline__ s16x4 lds_tr(u32 off) { return __builtin_amdgcn_ds_read_tr16_b64_v4i16((AS3 s16x4*)off); }
__device__ __forceinline__ void st16(rsrc_t r, u32 voff, u32 soff, u32x4 v) { __builtin_amdgcn_raw_buffer_store_b128(v, r, voff + soff, 0, 16); }
__device__ __forceinline__ u32x4 ld16(rsrc_t r, u32 voff, u32 soff) { return __builtin_amdgcn_raw_buffer_load_b128(r, voff, soff, 16); }

struct Args {
  const u16 *x, *k, *v, *shk, *ssm_k, *ssm_D, *w1, *w2, *w3, *w4;
  char* ws;
  u16* out;
};

struct Ctx {
  rsrc_t ws;
  u64 tag;
  int lane, w;
};
__device__ __forceinline__ int fresh(int v) {
  asm volatile("" : "+v"(v));
  return v;
}
__device__ __forceinline__ u32 foff(u32 f) { return WS_FL + f * 128; }
__device__ __forceinline__ void set_flag(const Ctx& c, u32 f, bool on = true) {
  const u32x2 t = {(u32)c.tag, (u32)(c.tag >> 32)};
  asm volatile("buffer_store_dwordx2 %0, %1, %2, %3 offen sc1" ::"v"(t), "v"((c.lane == 0 && on) ? 0u : OOB), "s"(c.ws), "s"(foff(f)) : "memory");
}
__device__ __forceinline__ u32x4 tagged(const Ctx& c, float x, float y) {
  return u32x4{__float_as_uint(x), (u32)c.tag, __float_as_uint(y), (u32)c.tag};
}
__device__ __forceinline__ bool stale(const Ctx& c, u32x4 v) { return v[1] != (u32)c.tag || v[3] != (u32)c.tag; }
template <bool SC1>
__device__ __forceinline__ u32x4 ald16(rsrc_t r, u32 voff, u32 soff) {
  u32x4 v;
  if (SC1) asm volatile("buffer_load_dwordx4 %0, %1, %2, %3 offen sc1" : "=v"(v) : "v"(voff), "s"(r), "s"(soff) : "memory");
  else asm volatile("buffer_load_dwordx4 %0, %1, %2, %3 offen" : "=v"(v) : "v"(voff), "s"(r), "s"(soff) : "memory");
  return v;
}
__device__ __forceinline__ u32 ald4(rsrc_t r, u32 voff, u32 soff) {
  u32 v;
  asm volatile("buffer_load_dword %0, %1, %2, %3 offen sc1" : "=v"(v) : "v"(voff), "s"(r), "s"(soff) : "memory");
  return v;
}
#define LANDED(v) asm volatile("" : "+v"(v))
__device__ __forceinline__ f32x4 ln_par(float mean, float var) {
  const float m1 = rbf(mean), sd1 = rbf(__builtin_amdgcn_sqrtf(rbf(rbf(var) + 1e-5f)));
  const float rs1 = __builtin_amdgcn_rcpf(sd1);
  const float m2 = rbf((mean - m1) * rs1), sd2 = rbf(__builtin_amdgcn_sqrtf(rbf(rbf(var * rs1 * rs1) + 1e-5f)));
  return f32x4{rs1, -m1 * rs1, -__builtin_fmaf(m2, sd1, m1), sd1 * sd2};
}
#define PSL 4
__device__ __forceinline__ u32x2 peek(const Ctx& c, u32 off) {
  u32x2 v;
  asm volatile("buffer_load_dwordx2 %0, %1, %2, 0 offen sc1" : "=v"(v) : "v"(off), "s"(c.ws) : "memory");
  return v;
}
template <int N>
__device__ __forceinline__ void check(const Ctx& c, u32x2 v, u32 off) {
  asm volatile("s_waitcnt vmcnt(%1)" : "+v"(v) : "n"(N) : "memory");
#pragma unroll 1
  for (int n = 0; n < (1 << 14) && __builtin_amdgcn_ballot_w64(__builtin_bit_cast(u64, v) != c.tag && off != OOB) != 0; ++n) {
    __builtin_amdgcn_s_sleep(PSL);
    v = peek(c, off);
    asm volatile("s_waitcnt vmcnt(0)" : "+v"(v)::"memory");
  }
}
__device__ __forceinline__ void poll(const Ctx& c, u32 off) { check<0>(c, peek(c, off), off); }
__device__ __forceinline__ u32 arrive(const Ctx& c, u32 e) {
  u32 old = 0;
  if (c.lane == 0) old = __hip_atomic_fetch_add((AS3 u32*)(L_CNT + 16 * e), 1u, __ATOMIC_RELAXED, __HIP_MEMORY_SCOPE_WORKGROUP);
  return __builtin_amdgcn_readfirstlane(old);
}
__device__ __forceinline__ void signal(const Ctx& c, u32 e) {
  asm volatile("" ::: "memory");
  LDS(volatile u32, L_CNT + 16 * e + 4 * c.w) = 1u;
}
__device__ __forceinline__ void signal_all(u32 e) {
  asm volatile("" ::: "memory");
  LDS(volatile u32x4, L_CNT + 16 * e) = u32x4{1u, 1u, 1u, 1u};
}
template <int N, int SLEEP>
__device__ __forceinline__ void await(u32 e) {
#pragma unroll 1
  for (int n = 0; n < (1 << 19); ++n) {
    u32x4 v = LDS(volatile u32x4, L_CNT + 16 * e);
    if (N == 2) v &= LDS(volatile u32x4, L_CNT + 16 * e + 16);
    if (__builtin_amdgcn_readfirstlane(v[0] & v[1] & v[2] & v[3])) break;
    if (SLEEP) __builtin_amdgcn_s_sleep(SLEEP);
  }
  asm volatile("" ::: "memory");
}
#define TAG_WAIT(c) do { PIN(); (c).tag += 1; asm volatile("s_waitcnt lgkmcnt(0)" : "+s"((c).tag) :: "memory"); } while (0)

__device__ __forceinline__ void side_stats(const Args& a, Ctx& c, int xcd, int j16) {
  STAMP_DECL; STAMP(0);
  const int lane = c.lane, w = c.w;
  const int row0 = xcd * 256 + j16 * 16 + 4 * w, col0 = (xcd & 1) * 512 + j16 * 32;
  const u16* xp = a.x + (size_t)(row0 + (lane >> 4)) * D + 8 * (lane & 15);
  const u16* wp = a.w1 + (size_t)((xcd >> 1) * 256 + 16 * w + (lane >> 2)) * D + col0 + 8 * (lane & 3);
  u32x4 xr[8], wr[4];
#pragma unroll
  for (int i = 0; i < 8; ++i) {
    xr[i] = *(const u32x4*)(xp + 128 * i);
    if (i < 4) wr[i] = *(const u32x4*)(wp + (size_t)64 * i * D);
  }
  PIN();
  if (w == 0 && lane < E_N) LDS(u32x4, L_CNT + 16 * lane) = u32x4{0u, 0u, 0u, 0u};
  TAG_WAIT(c);
  BAR();
  PIN();
  const u32 o11 = sconst(0x3F803F80u), o01 = sconst(0x00003F80u), o10 = sconst(0x3F800000u);
  float s1 = 0.f, s2 = 0.f, cs[8];
#pragma unroll
  for (int e = 0; e < 8; ++e) cs[e] = 0.f;
#pragma unroll
  for (int i = 0; i < 8; ++i) {
#pragma unroll
    for (int e = 0; e < 4; ++e) {
      s1 = dot2(xr[i][e], o11, s1);
      s2 = dot2(xr[i][e], xr[i][e], s2);
      if (i < 4) {
        cs[2 * e] = dot2(wr[i][e], o01, cs[2 * e]);
        cs[2 * e + 1] = dot2(wr[i][e], o10, cs[2 * e + 1]);
      }
    }
    PIN();
  }
  STAMP(1);
  s1 = dpp_add<0x140>(dpp_add<0x141>(dpp_add<0x4E>(dpp_add<0xB1>(s1))));
  s2 = dpp_add<0x140>(dpp_add<0x141>(dpp_add<0x4E>(dpp_add<0xB1>(s2))));
  const float mean = s1 * (1.0f / D);
  const float var = fmaxf(s2 * (1.0f / D) - mean * mean, 0.f);
  st16(c.ws, (lane & 15) == 0 ? lane : 0x80000000u, WS_XS + row0 * 16, tagged(c, mean, var));
  const float u0 = red32(cs[0], cs[4]), u1 = red32(cs[1], cs[5]), u2 = red32(cs[2], cs[6]), u3 = red32(cs[3], cs[7]);
  const float t0 = dpp_add<0x128>(dpp_add<0x124>(red16(u0, u2))), t1 = dpp_add<0x128>(dpp_add<0x124>(red16(u1, u3)));
  if ((lane & 12) == 0) LDS(f32x2, L_T3 + w * 128 + ((lane & 3) * 4 + (lane >> 4)) * 8) = f32x2{t0, t1};
  asm volatile("" ::: "memory");
  if (arrive(c, E_SUM) == 3) {
    asm volatile("" ::: "memory");
    const u32 o = L_T3 + (lane & 15) * 8;
    const f32x2 t = (LDS(f32x2, o) + LDS(f32x2, o + 128)) + (LDS(f32x2, o + 256) + LDS(f32x2, o + 384));
    st16(c.ws, lane < 16 ? lane * 16 : 0x80000000u, WS_C1 + (((xcd & 1) * 16 + j16) * 4 + (xcd >> 1)) * 256, tagged(c, t[0], t[1]));
  }
  STAMP(2);
  u32x4 hc[2][4];
#pragma unroll
  for (int q = 0; q < 2; ++q)
#pragma unroll
    for (int i = 0; i < 4; ++i) hc[q][i] = *(const u32x4*)(a.ssm_k + (size_t)((xcd * 16 + j16) * 8 + w + 4 * q) * S + 8 * (i * 64 + lane));
  PIN();
#pragma unroll
  for (int q = 0; q < 2; ++q) {
    const u32 hb = (w + 4 * q) * CV_CH;
    if (lane < 56) LDS(u32x4, hb + (lane < 28 ? lane * 16 : CV_H + (lane - 28) * 16)) = u32x4{0u, 0u, 0u, 0u};
#pragma unroll
    for (int i = 0; i < 4; ++i) {
      u32x4 o;
      const u32 prev = __builtin_amdgcn_update_dpp(i ? __builtin_amdgcn_readlane(hc[q][i ? i - 1 : 0][3], 63) : 0u, hc[q][i][3], 0x138, 0xf, 0xf, false);
      o[0] = __builtin_amdgcn_alignbit(hc[q][i][0], prev, 16);
      o[1] = __builtin_amdgcn_alignbit(hc[q][i][1], hc[q][i][0], 16);
      o[2] = __builtin_amdgcn_alignbit(hc[q][i][2], hc[q][i][1], 16);
      o[3] = __builtin_amdgcn_alignbit(hc[q][i][3], hc[q][i][2], 16);
      LDS(u32x4, hb + 448 + (i * 64 + lane) * 16) = o;
      LDS(u32x4, hb + CV_H + 448 + (i * 64 + lane) * 16) = hc[q][i];
    }
  }
  STAMP(3);
  STAMP_OUT(a.ws, 0);
}

#define KV_SLEEP 0
__device__ __forceinline__ void side_kv(const Args& a, Ctx& c, int xcd, int j16) {
  STAMP_DECL; STAMP(0);
  const int lane = c.lane, w = c.w;
#pragma unroll
  for (int i = 0; i < 2; ++i)
    touch(mk_rsrc(a.w1, D * D * 2), L_DUM + w * 256, (lane >> 3) * 2048 + (lane & 7) * 128, (j16 * 64 + w * 16 + 8 * i) * 2048 + (xcd & 1) * 1024);
  touch(mk_rsrc(a.x, S * D * 2), L_DUM + w * 256, lane * 128, ((xcd ^ 1) * 256 + j16 * 16 + 4 * w) * 2048);
  BAR();
  if (KV_SLEEP) __builtin_amdgcn_s_sleep(KV_SLEEP);
  const int cb = (xcd + j16) & 7, t0 = j16 * 128, d0 = cb * 128 + w * 32;
  const int tg = lane >> 2, dc = lane & 3;
  const int tb = t0 + 8 * tg, dd = d0 + 8 * dc;
  u32x4 kr[11], vr[8], sh[4];
#pragma unroll
  for (int i = 0; i < 4; ++i) sh[i] = *(const u32x4*)(a.shk + (size_t)dd * 4 + 8 * i);
#pragma unroll
  for (int i = 3; i < 11; ++i) kr[i] = *(const u32x4*)(a.k + (size_t)(tb - 3 + i) * D + dd);
#pragma unroll
  for (int i = 0; i < 8; ++i) vr[i] = *(const u32x4*)(a.v + (size_t)(tb + i) * D + dd);
#pragma unroll
  for (int i = 0; i < 3; ++i) kr[i] = *(const u32x4*)(a.k + (size_t)(tb == 0 ? 0 : tb - 3 + i) * D + dd);
  PIN();
  if (w < 2) touch(mk_rsrc(a.w2, D * D * 2), L_DUM + w * 256, (lane >> 3) * 2048 + (lane & 7) * 128, ((xcd >> 1) * 256 + j16 * 16 + 8 * w) * 2048 + (xcd & 1) * 1024);
  TAG_WAIT(c);
  if (tb == 0) {
#pragma unroll
    for (int i = 0; i < 3; ++i) kr[i] = u32x4{0u, 0u, 0u, 0u};
  }
  const u32 T = (w + 4) * CV_CH + 2 * CV_H;
#pragma unroll
  for (int e = 0; e < 4; ++e) {
    const f32x2 c0 = {bf_lo(sh[e][0]), bf_lo(sh[e][2])}, c1 = {bf_hi(sh[e][0]), bf_hi(sh[e][2])};
    const f32x2 c2 = {bf_lo(sh[e][1]), bf_lo(sh[e][3])}, c3 = {bf_hi(sh[e][1]), bf_hi(sh[e][3])};
    f32x2 kf[11], o[8];
#pragma unroll
    for (int i = 0; i < 11; ++i) kf[i] = bf_2(kr[i][e]);
#pragma unroll
    for (int i = 0; i < 8; ++i) o[i] = fma2(kf[i + 3], c0, fma2(kf[i + 2], c1, fma2(kf[i + 1], c2, kf[i] * c3))) * bf_2(vr[i][e]);
#pragma unroll
    for (int h = 0; h < 2; ++h)
      LDS(u32x4, T + (8 * dc + 2 * e + h) * 256 + (((15 - tg) ^ (2 * dc)) * 16)) =
          u32x4{pack2(o[7][h], o[6][h]), pack2(o[5][h], o[4][h]), pack2(o[3][h], o[2][h]), pack2(o[1][h], o[0][h])};
  }
  STAMP(1);
#pragma unroll
  for (int q = 0; q < 8; ++q)
    st16(c.ws, (lane >> 4) * (S * 2) + (lane & 15) * 16, WS_UR + (d0 + 4 * q) * (S * 2) + (S - 128 - t0) * 2,
         LDS(u32x4, T + (4 * q + (lane >> 4)) * 256 + (((lane & 15) ^ (q & 6)) * 16)));
  WAIT_VM(0);
  signal(c, E_KV);
  STAMP(2);
  if (w == 0) {
    await<1, 1>(E_KV);
    set_flag(c, F_KV + cb * 16 + j16);
    poll(c, lane < 16 ? foff(F_KV + xcd * 16 + lane) : OOB);
  }
  STAMP(3);
  STAMP_OUT(a.ws, 6);
}

#define CV_SLEEP 8
__device__ __forceinline__ void conv(const Args& a, const Ctx& c, int w8, int xcd, int j16) {
  STAMP_DECL; STAMP(0);
  const int lane = fresh(c.lane);
  const int u = xcd * 16 + j16, ch = u * 8 + w8;
  const u32 hb = w8 * CV_CH, ub = hb + 2 * CV_H;
  const float dcoef = bf_lo((u32)a.ssm_D[ch]);
  if (lane < 8) LDS(u32x4, ub + (lane >> 2) * CV_U + 4096 + (lane & 3) * 16) = u32x4{0u, 0u, 0u, 0u};
  WAIT_LGKM0();
  BAR();
#pragma unroll
  for (int i = 0; i < 4; ++i) {
    glds16<true>(c.ws, ub + CV_U + i * 1024, lane * 16, WS_UR + ch * (S * 2) + i * 1024);
    glds16<true>(c.ws, ub + i * 1024, lane * 16, WS_UR + ch * (S * 2) + i * 1024 + 4);
  }
  const int jc = j16 < 14 ? j16 : 13, rb = (xcd >> 2) * 2 + (jc & 1), cb = (xcd & 3) * 896 + (jc >> 1) * 128;
  WAIT_VM(0);
  if (lane == 0) LDS(u32, ub + 4092) = 0u;
  if (w8 >= 4) __builtin_amdgcn_s_sleep(CV_SLEEP);
  STAMP(1);
  const int r16 = lane & 15, g = lane >> 4;
  const u32 aaddr = hb + (r16 & 1) * CV_H + 64 * (r16 >> 1) + 16 * g;
  const u32 baddr = ub + (r16 & 1) * CV_U + 56 - 8 * (r16 >> 1) + 16 * g;
  f32x4 acc[8];
#pragma unroll
  for (int b = 0; b < 8; ++b) acc[b] = f32x4{0.f, 0.f, 0.f, 0.f};
  auto pass = [&](int r) {
    bf16x8 A[8], B[8];
#pragma unroll
    for (int p = 0; p < 8; ++p) {
      A[p] = LDS(bf16x8, aaddr + 64 * r + 512 * p);
      const u32x2 lo = LDS(volatile u32x2, baddr + 64 * r + 512 * (7 - p));
      const u32x2 hi = LDS(volatile u32x2, baddr + 64 * r + 512 * (7 - p) + 8);
      B[p] = __builtin_bit_cast(bf16x8, u32x4{lo[0], lo[1], hi[0], hi[1]});
    }
#pragma unroll
    for (int s = 0; s < 8; ++s)
#pragma unroll
      for (int p = 0; p <= s; ++p) acc[s] = __builtin_amdgcn_mfma_f32_16x16x32_bf16(A[p], B[s - p], acc[s], 0, 0, 0);
  };
#pragma unroll 1
  for (int r = 0; r < 7; ++r) {
    if (r == (w8 & 3))
      touch(mk_rsrc(a.w3, D * F * 2), L_DUM + w8 * 256, (((lane >> 1) & 3) + 32 * (lane >> 3)) * (F * 2) + (lane & 1) * 128, (rb * 256 + 4 * w8) * (F * 2) + cb * 2);
    if (r == 3 + (w8 & 3) && w8 < 7) touch(mk_rsrc(a.w4, F * D * 2), L_DUM + w8 * 256, lane * 128, (28 * u + 4 * w8) * 2048);
    pass(r);
  }
  PIN();
  u32x4 wr[8];
#pragma unroll
  for (int i = 0; i < 8; ++i) wr[i] = *(const u32x4*)(a.w3 + (size_t)(rb * 256 + 4 * w8 + (lane >> 4) + 32 * i) * F + cb + 8 * (lane & 15));
  PIN();
  pass(7);
  STAMP(2);
#pragma unroll
  for (int b = 0; b < 8; ++b) {
    const u32 o = hb + 512 * b + 4 * r16 + 128 * g;
    LDS(u32, o) = pack2(acc[b][0], acc[b][1]);
    LDS(u32, o + 64) = pack2(acc[b][2], acc[b][3]);
  }
#pragma unroll
  for (int i = 0; i < 4; ++i) {
    const int n = i * 64 + lane;
    const u32x4 y = LDS(u32x4, hb + n * 16);
    const u32x4 uu = LDS(u32x4, ub + CV_U + (255 - n) * 16);
    u32x4 o;
#pragma unroll
    for (int e = 0; e < 4; ++e)
      o[e] = pack2(__builtin_fmaf(bf_hi(uu[3 - e]), dcoef, bf_lo(y[e])), __builtin_fmaf(bf_lo(uu[3 - e]), dcoef, bf_hi(y[e])));
    st16(c.ws, lane * 16, WS_YT + ch * (S * 2) + i * 1024, o);
  }
  PIN();
  {
    const u32 o01 = sconst(0x00003F80u), o10 = sconst(0x3F800000u);
    float cs[8];
#pragma unroll
    for (int e = 0; e < 8; ++e) {
      cs[e] = 0.f;
#pragma unroll
      for (int i = 0; i < 8; ++i) cs[e] = dot2(wr[i][e >> 1], (e & 1) ? o10 : o01, cs[e]);
    }
    const float u0 = red32(cs[0], cs[4]), u1 = red32(cs[1], cs[5]), u2 = red32(cs[2], cs[6]), u3 = red32(cs[3], cs[7]);
    LDS(f32x2, L_TB + w8 * 512 + r16 * 32 + g * 8) = f32x2{red16(u0, u2), red16(u1, u3)};
  }
  WAIT_LGKM0();
  BAR();
  if (w8 < 2 && j16 < 14) {
    float t = 0.f;
#pragma unroll
    for (int i = 0; i < 8; ++i) t += LDS(float, L_TB + i * 512 + w8 * 256 + lane * 4);
    __builtin_amdgcn_raw_buffer_store_b32(__float_as_uint(t), c.ws, lane * 4, WS_C3 + (rb * F + cb + w8 * 64) * 4, 16);
  }
  STAMP(3);
  STAMP_OUT(a.ws, 3);
}

template <int BN> struct WL;
template <> struct WL<128> { static constexpr int BYTES = 16 * 1056, HI = 256, KK = 8 * 1056; };
template <> struct WL<224> { static constexpr int BYTES = 4 * 7200, HI = 4 * 448, KK = 2 * 7200; };
constexpr int AP1 = 1088, AB1 = 16 * AP1, AB = 16384;
constexpr int ST1 = AB1 + WL<128>::BYTES, ST2 = AB + WL<128>::BYTES, ST3 = AB + WL<224>::BYTES;
static_assert(4 * ST1 <= (int)L_TB && 3 * ST3 <= (int)L_TB, "ring");
#define TAIL 8
constexpr int NFS = 28 - TAIL / 2;

template <int NT> struct Frag { bf16x8 a[4]; s16x4 w[NT][2]; };

template <int BN, int MT, int AI>
__device__ __forceinline__ void rd(Frag<BN / 32>& f, int r, u32 ab, u32 wb) {
  constexpr int NT = BN / 32;
  if (r == 2) f.a[0] = LDS(bf16x8, ab);
  else if (r > 2 * NT) f.a[r - 2 * NT] = LDS(bf16x8, ab + (r - 2 * NT) * AI);
  else {
    const int z = r > 2 ? r - 1 : r;
    f.w[z >> 1][z & 1] = lds_tr(wb + (z >> 1) * 32 + (z & 1) * WL<BN>::HI);
  }
}
template <int BN, int MT, int AI, bool LOAD, class Late>
__device__ __forceinline__ void half(f32x4 (&acc)[4][BN / 32], const Frag<BN / 32>& c, Frag<BN / 32>& l, u32 ab, u32 wb, Late&& late) {
  constexpr int NT = BN / 32, NR = MT + 2 * NT, RPG = MT == 4 ? 1 : 2;
#pragma unroll
  for (int m = 0; m < MT * NT; ++m) {
    const int i = m / NT, j = m % NT;
    acc[i][j] = __builtin_amdgcn_mfma_f32_16x16x32_bf16(
        __builtin_bit_cast(bf16x8, __builtin_shufflevector(c.w[j][0], c.w[j][1], 0, 1, 2, 3, 4, 5, 6, 7)), c.a[i], acc[i][j], 0, 0, 0);
    PIN();
    if (LOAD) {
#pragma unroll
      for (int r = m * RPG; r < (m + 1) * RPG && r < NR; ++r) rd<BN, MT, AI>(l, r, ab, wb);
    }
    if (m == (NR + RPG - 1) / RPG) late();
    PIN();
  }
}
template <int BN, int MT, int AI, int AH, int STAGE, int NS, int MODE_>
__device__ __forceinline__ void step(f32x4 (&acc)[4][BN / 32], Frag<BN / 32>& f0, Frag<BN / 32>& f1, u32& ab, u32& wb, int& st, u32 ev = 0) {
  constexpr int MODE = MODE_ & 3;
  st = (st + 1 == NS) ? 0 : st + 1;
  const int dl = st == 0 ? -(NS - 1) * STAGE : STAGE;
  half<BN, MT, AI, true>(acc, f0, f1, ab + AH, wb + WL<BN>::KK, [&]() {
    ab += dl;
    wb += dl;
    asm volatile("" : "+v"(ab), "+v"(wb));
  });
  WAIT_LGKM0();
  if (MODE_ & 4) WAIT_VM(0);
  if (!(MODE & 1)) BAR();
  if (MODE == 1) LDS(volatile u32, ev) = 1u;
  PIN();
  half<BN, MT, AI, (MODE < 2)>(acc, f1, f0, ab, wb, [&]() {});
  if (MODE < 2) WAIT_LGKM0();
}

__device__ __forceinline__ u32 gelu2(f32x2 t, float c0, float c1, float sd) {
  const f32x2 z = t * fma2(t * t, dup2(c1), dup2(c0));
  const f32x2 e = {__builtin_amdgcn_exp2f(z[0]), __builtin_amdgcn_exp2f(z[1])};
  const f32x2 d = fma2(e, dup2(sd), dup2(sd));
  const f32x2 o = t * f32x2{__builtin_amdgcn_rcpf(d[0]), __builtin_amdgcn_rcpf(d[1])};
  return pack2(o[0], o[1]);
}
__device__ __forceinline__ u32x4 gelu16(f32x4 x, f32x4 y, f32x4 cs, f32x4 p, f32x4 q) {
  const u32 x01 = gelu2(fma2(lo2(cs), dup2(p[0]), lo2(x)), p[2], p[3], p[1]);
  const u32 x23 = gelu2(fma2(hi2(cs), dup2(p[0]), hi2(x)), p[2], p[3], p[1]);
  const u32 y01 = gelu2(fma2(lo2(cs), dup2(q[0]), lo2(y)), q[2], q[3], q[1]);
  const u32 y23 = gelu2(fma2(hi2(cs), dup2(q[0]), hi2(y)), q[2], q[3], q[1]);
  return swap16(x01, x23, y01, y23);
}
__device__ __forceinline__ f32x4 gelu_par(float nmean, float sd) {
  const float rs = __builtin_amdgcn_rcpf(sd);
  return f32x4{nmean, sd, -2.302208198f * rs, -0.1029432397f * rs * rs * rs};
}

struct Order {
  int cls, nxt;
  __device__ __forceinline__ Order(int c) : cls(c), nxt(c == 0 ? 1 : 0) {}
  __device__ __forceinline__ int operator()(int u) {
    if (u == 0) return cls;
    if (((u - 1) & 3) == cls && u + 3 < 16) return u + 3;
    const int kt = nxt;
    nxt += ((nxt + 1) & 3) == cls ? 2 : 1;
    return kt;
  }
};

struct Loader {
  rsrc_t rA, rW;
  u32 voA, voW, sa0, sa1, sw, la, lw, lb;
  int tab, n;
  __device__ __forceinline__ void W(int h, u32 k) {
    glds16<false>(rW, lb + lw + h * 8 * 1056, voW, sw + (k << 15));
    glds16<false>(rW, lb + lw + h * 8 * 1056 + 4 * 1056, voW, sw + (k << 15) + 8 * 2048);
  }
  template <bool BOTH>
  __device__ __forceinline__ void A(int h, u32 k) {
    glds16<true>(rA, lb + la + h * 8192, voA, sa0 + (k << 16));
    if (BOTH) glds16<true>(rA, lb + la + h * 8192 + 1024, voA, sa1 + (k << 16));
  }
  template <int M>
  __device__ __forceinline__ void issue() {
    const u32 k0 = __builtin_amdgcn_readlane(tab, n), k1 = __builtin_amdgcn_readlane(tab, n + 1);
    if (M & 2) A<!(M & 8)>(0, k0);
    if (M & 4) A<!(M & 8)>(1, k1);
    if (M & 1) {
      W(0, k0);
      W(1, k1);
    }
    n += 2;
    lb = (lb == 3 * ST2) ? 0 : lb + ST2;
  }
  __device__ __forceinline__ void seek(int stage) {
    n = 2 * stage;
    lb = (stage & 3) * ST2;
  }
};

struct Loader3 {
  rsrc_t rA, rW;
  u32 voA, voW[7], sa, sw, la, lw;
  __device__ __forceinline__ void init(const Args& a, const Ctx& c, int xcd, int idx) {
    const int lane = fresh(c.lane), w = c.w;
    rA = c.ws;
    rW = mk_rsrc(a.w3, D * F * 2);
    voA = lane * 16;
#pragma unroll
    for (int i = 0; i < 7; ++i) {
      const int l = i * 64 + lane, r = l / 28, q = l % 28, wn = q / 14, cc = q % 14;
      voW[i] = ((r & 7) + 16 * (r >> 3)) * (F * 2) + (cc < 12 ? wn * 12 + cc : 12 + 2 * wn + cc) * 16;
    }
    sa = WS_X1 + w * 65536 + ((xcd >> 2) * 8 + (idx >> 2)) * 4096;
    sw = (8 * (w & 1) + 32 * (w >> 1)) * (F * 2) + ((xcd & 3) * 4 + (idx & 3)) * 448;
    la = w * 4096;
    lw = AB + w * 7200;
  }
  __device__ __forceinline__ void W(u32 lb, int kt) {
#pragma unroll
    for (int i = 0; i < 7; ++i) glds16<false>(rW, lb + lw + i * 1024, voW[i], sw + kt * (64 * F * 2));
  }
  __device__ __forceinline__ void A(u32 lb, int kt) {
#pragma unroll
    for (int i = 0; i < 4; ++i) glds16<true>(rA, lb + la + i * 1024, voA, sa + kt * 262144 + i * 1024);
  }
};

__device__ __forceinline__ void load12(const Args& a, Ctx& c, int xcd, int idx, Loader3& L3) {
  STAMP_DECL; STAMP(0);
  TR_DECL;
  const int lane = c.lane, w = c.w;
  const int tm = (xcd >> 1) * 4 + (idx >> 2), tn = (xcd & 1) * 4 + (idx & 3);
  const int m0 = tm * 128, n0 = tn * 128;
  const u32 voW = (4 * ((lane >> 4) & 1) + 16 * (lane >> 5)) * (D * 2) + (lane & 15) * 16;
  Loader L;
  {
    const rsrc_t rA = mk_rsrc(a.x, S * D * 2), rW = mk_rsrc(a.w1, D * D * 2);
    const int sft = ((tn >> 2) + 1) * ((tn & 2) ? 1 : -1);
    u32 voA[4];
#pragma unroll
    for (int p = 0; p < 4; ++p) {
      const int r = m0 + sft + 32 * p + 8 * w + (lane >> 3);
      voA[p] = min(max(r, 0), S - 1) * (D * 2) + (((lane & 7) ^ (lane >> 4)) * 16);
    }
    Order ord(((idx >> 2) + idx) & 3);
    u32 lb = 0;
    auto issue = [&](int u) {
      const int kt = ord(u);
      const u32 sw = (kt * 64 + w) * (D * 2) + n0 * 2;
#pragma unroll
      for (int i = 0; i < 4; ++i) {
        glds16<false>(rW, lb + AB1 + (4 * i + w) * 1056, voW, sw + (8 * (i & 1) + 32 * (i >> 1)) * (D * 2));
        glds16<false>(rA, lb + (4 * i + w) * AP1, voA[i], kt * 128);
      }
      lb = (lb == 3 * ST1) ? 0 : lb + ST1;
    };
    issue(0);
    issue(1);
    LWAIT(8);
    LBAR();
    issue(2);
#pragma unroll 1
    for (int k = 0; k <= 12; ++k) {
      issue(k + 3);
      LWAIT(16);
      LBAR();
    }
    const u32 ro = WS_XS + min(max(min(m0 - 2 + 64 * w + lane, m0 + 129), 0), S - 1) * 16;
    const u32 co = WS_C1 + (tn * 4 + w) * 1024 + lane * 16;
    u32x4 rv = ald16<true>(c.ws, ro, 0), cv = ald16<true>(c.ws, co, 0);
    L.rA = c.ws;
    L.rW = mk_rsrc(a.w2, D * D * 2);
    L.voA = lane * 16;
    L.voW = voW;
    L.sa0 = WS_H + (w & 1) * 65536 + (m0 + (w >> 1) * 64) * 32;
    L.sa1 = L.sa0 + 1024;
    L.sw = w * (D * 2) + n0 * 2;
    L.la = (w & 1) * 4096 + (w >> 1) * 2048;
    L.lw = AB + w * 1056;
    {
      const int m = lane < 18 ? lane - 4 : lane - 18;
      L.tab = lane < 4 ? tn * 8 + 2 * lane : ((tn + 1 + (m >> 1)) & 7) * 8 + 4 * (m & 1) + (lane < 18 ? 0 : 2);
    }
    L.seek(0);
    L.issue<1>();
    LWAIT(14);
    LBAR();
    L.issue<1>();
    LWAIT(10);
    LBAR();
    L.issue<1>();
    TAG_WAIT(c);
    LWAIT(12);
    LANDED(rv);
    LANDED(cv);
#pragma unroll 1
    for (int n = 0; n < (1 << 14) && __builtin_amdgcn_ballot_w64(stale(c, rv) || stale(c, cv)) != 0; ++n) {
      rv = ald16<true>(c.ws, ro, 0);
      cv = ald16<true>(c.ws, co, 0);
      WAIT_VM(0);
      LANDED(rv);
      LANDED(cv);
    }
    if (w < 3) LDS(f32x4, L_TB + w * 1024 + lane * 16) = ln_par(__uint_as_float(rv[0]), __uint_as_float(rv[2]));
    const float t = red32(__uint_as_float(cv[0]), __uint_as_float(cv[2]));
    LDS(float, L_TB + 3072 + (32 * w + 2 * (lane & 15) + (lane >> 5)) * 4) = red16(t, t);
    WAIT_LGKM0();
    LBAR();
  }
  L.issue<1>();
  LWAIT(8);
  signal(c, E_2L);
  L3.init(a, c, xcd, idx);
  STAMP(1);
  if (w == 0) {
    await<1, 1>(E_HE);
    set_flag(c, F_HE + tm * 8 + tn);
    poll(c, lane < 8 ? foff(F_HE + tm * 8 + lane) : OOB);
    signal_all(E_HGO);
  } else {
    await<1, 1>(E_HGO);
  }
  STAMP(2);
  L.seek(2);
  L.issue<6>();
  L.issue<6>();
  await<1, 0>(E_2S);
  L.issue<7>();
  LWAIT(12);
  STAMP(3);
  LBAR();
  set_flag(c, F_HL + tm * 8 + tn, w == 0);
  L.issue<7>();
  LWAIT(17);
  LBAR();
  L.issue<7>();
  LWAIT(16);
  LBAR();
  const u32 fo = (w == 0 && lane < 8) ? foff(F_HL + tm * 8 + lane) : OOB;
  const u32x2 fv = peek(c, fo);
  L.issue<7>();
  LWAIT(17);
  LBAR();
  L.issue<7>();
  TRA();
  check<16>(c, fv, fo);
  LBAR();
#pragma unroll 1
  for (int k = 6; k <= 12; ++k) {
    L.issue<7>();
    LWAIT(16);
    LBAR();
  }
  Order ord(((idx >> 2) + idx) & 3);
  LWAIT(8);
  LBAR();
  L3.W(0, ord(0));
  LWAIT(7);
  LBAR();
  L3.W(ST3, ord(1));
  LBAR();
  if (w == 0) {
    await<1, 1>(E_X1);
    set_flag(c, F_X1 + tm * 8 + tn);
  }
  STAMP_OUT(a.ws, 7);
  TR_OUT(a.ws, 0);
}

__device__ __forceinline__ void cons12(const Args& a, Ctx& c, int xcd, int idx) {
  const int lane = c.lane, w = c.w;
  const int tm = (xcd >> 1) * 4 + (idx >> 2), tn = (xcd & 1) * 4 + (idx & 3);
  const int m0 = tm * 128, n0 = tn * 128;
  const int wm = w >> 1, wn = w & 1, r16 = lane & 15, g = lane >> 4;
  const u32 wb0 = wn * 128 + 8 * (lane & 3) + ((r16 >> 2) + 4 * (g & 1)) * 1056 + (g >> 1) * 512;
  const u32 vo = (16 * (g & 1) + r16) * 32 + (g >> 1) * 16;
  f32x4 acc[4][4];
  Frag<4> f0, f1;
  u32x4 xr[4][2];
  {
    STAMP_DECL; STAMP(0);
    if (w == 0 && lane < E_N) LDS(u32x4, L_CNT + 16 * lane) = u32x4{0u, 0u, 0u, 0u};
    u32 ab = (wm * 8 + (r16 >> 3)) * AP1 + (r16 & 7) * 128 + ((g ^ ((r16 >> 1) & 3)) * 16), wb = AB1 + wb0;
#pragma unroll
    for (int i = 0; i < 4; ++i)
#pragma unroll
      for (int j = 0; j < 4; ++j) {
        acc[i][j] = f32x4{0.f, 0.f, 0.f, 0.f};
        asm volatile("" : "+v"(acc[i][j]));
      }
    TAG_WAIT(c);
    BAR();
    STAMP(1);
#pragma unroll
    for (int r = 0; r < 12; ++r) rd<128, 4, 2 * AP1>(f0, r, ab, wb);
    WAIT_LGKM0();
    PIN();
    int st = 0;
#pragma unroll 1
    for (int kt = 0; kt < 15; ++kt) step<128, 4, 2 * AP1, 64, ST1, 4, 0>(acc, f0, f1, ab, wb, st);
#pragma unroll
    for (int i = 0; i < 4; ++i)
#pragma unroll
      for (int j = 0; j < 2; ++j)
        xr[i][j] = *(const u32x4*)(a.x + (size_t)(m0 + wm * 64 + 16 * i + r16) * D + n0 + wn * 64 + 32 * j + 16 * (g & 1) + 8 * (g >> 1));
    PIN();
    step<128, 4, 2 * AP1, 64, ST1, 4, 2>(acc, f0, f1, ab, wb, st);
    STAMP(2);
    const int sft = ((tn >> 2) + 1) * ((tn & 2) ? 1 : -1);
    f32x4 p[4];
#pragma unroll
    for (int i = 0; i < 4; ++i) {
      f32x2 ms = LDS(f32x2, L_TB + (2 + sft + wm * 64 + 16 * i + r16) * 16 + 8);
      if ((i == 0 || i == 3) && (tm == 0 || tm == 15)) {
        const bool in = (u32)(m0 + sft + wm * 64 + 16 * i + r16) < (u32)S;
        ms = in ? ms : f32x2{0.f, 1.f};
#pragma unroll
        for (int j = 0; j < 4; ++j) acc[i][j] = in ? acc[i][j] : f32x4{0.f, 0.f, 0.f, 0.f};
      }
      p[i] = gelu_par(ms[0], ms[1]);
    }
#pragma unroll
    for (int j = 0; j < 4; ++j) {
      const f32x4 cs = LDS(f32x4, L_TB + 3072 + (wn * 64 + 16 * j + 4 * g) * 4);
#pragma unroll
      for (int ip = 0; ip < 2; ++ip) {
        const u32x4 v = gelu16(acc[2 * ip][j], acc[2 * ip + 1][j], cs, p[2 * ip], p[2 * ip + 1]);
        st16(c.ws, vo, WS_H + (tn * 8 + wn * 4 + j) * 65536 + (m0 + wm * 64 + ip * 32) * 32, v);
        LDS(u32x4, wn * ST2 + j * 4096 + (wm * 64 + ip * 32) * 32 + vo) = v;
      }
      if (j == 2) {
        WAIT_VM(2);
        signal(c, E_HE);
      }
    }
    STAMP(3);
    STAMP_OUT(a.ws, 1);
  }
  STAMP_DECL; STAMP(0);
  signal(c, E_2C);
#pragma unroll
  for (int i = 0; i < 4; ++i) {
    const f32x2 xs = LDS(f32x2, L_TB + (2 + wm * 64 + 16 * i + r16) * 16);
#pragma unroll
    for (int j = 0; j < 2; ++j) {
      const u32x4 xv = swap16(xr[i][j][0], xr[i][j][1], xr[i][j][2], xr[i][j][3]);
#pragma unroll
      for (int h = 0; h < 2; ++h) {
        const f32x2 l = fma2(bf_2(xv[2 * h]), dup2(xs[0]), dup2(xs[1])), u = fma2(bf_2(xv[2 * h + 1]), dup2(xs[0]), dup2(xs[1]));
        acc[i][2 * j + h] = f32x4{l[0], l[1], u[0], u[1]};
        asm volatile("" : "+v"(acc[i][2 * j + h]));
      }
    }
  }
  u32 ab = (g >> 1) * 4096 + (wm * 64 + r16) * 32 + (g & 1) * 16, wb = AB + wb0;
  await<2, 0>(E_2C);
  STAMP(1);
#pragma unroll
  for (int r = 0; r < 12; ++r) rd<128, 4, 512>(f0, r, ab, wb);
  WAIT_LGKM0();
  PIN();
  int st = 0;
  step<128, 4, 512, 8192, ST2, 4, 1>(acc, f0, f1, ab, wb, st, L_CNT + 16 * E_2S + 4 * w);
#pragma unroll 1
  for (int kt = 1; kt < 15; ++kt) step<128, 4, 512, 8192, ST2, 4, 4>(acc, f0, f1, ab, wb, st);
  step<128, 4, 512, 8192, ST2, 4, 2>(acc, f0, f1, ab, wb, st);
  STAMP(2);
#pragma unroll
  for (int j = 0; j < 4; ++j)
#pragma unroll
    for (int ip = 0; ip < 2; ++ip) {
      const f32x4 x = acc[2 * ip][j], y = acc[2 * ip + 1][j];
      st16(c.ws, vo, WS_X1 + (tn * 8 + wn * 4 + j) * 65536 + (m0 + wm * 64 + ip * 32) * 32,
           swap16(pack2(x[0], x[1]), pack2(x[2], x[3]), pack2(y[0], y[1]), pack2(y[2], y[3])));
    }
  PIN();
  float t1[4], t2[4];
#pragma unroll
  for (int i = 0; i < 4; ++i) {
    f32x2 s1 = lo2(acc[i][0]) + hi2(acc[i][0]), s2 = fma2(lo2(acc[i][0]), lo2(acc[i][0]), hi2(acc[i][0]) * hi2(acc[i][0]));
#pragma unroll
    for (int j = 1; j < 4; ++j) {
      s1 += lo2(acc[i][j]) + hi2(acc[i][j]);
      s2 = fma2(lo2(acc[i][j]), lo2(acc[i][j]), fma2(hi2(acc[i][j]), hi2(acc[i][j]), s2));
    }
    t1[i] = s1[0] + s1[1];
    t2[i] = s2[0] + s2[1];
  }
  const float r1 = red16(red32(t1[0], t1[2]), red32(t1[1], t1[3])), r2 = red16(red32(t2[0], t2[2]), red32(t2[1], t2[3]));
  __builtin_amdgcn_raw_buffer_store_b64(u32x2{__float_as_uint(r1), __float_as_uint(r2)}, c.ws, lane * 8,
                                        WS_ST + ((tm * 16 + tn * 2 + wn) * 128 + wm * 64) * 8, 16);
  WAIT_VM(1);
  signal(c, E_X1);
  STAMP(3);
  STAMP_OUT(a.ws, 2);
}

#define GEJ 3
#define TA 2
#define TB 5
__device__ __forceinline__ void load34(const Args& a, const Ctx& c, int xcd, int idx, Loader3& L3) {
  TR_DECL;
  const int lane = fresh(c.lane), w = c.w;
  const int tm = (xcd >> 2) * 8 + (idx >> 2), m0 = tm * 128, cls = ((idx >> 2) + idx) & 3;
  const int blk = (xcd & 1) * 4 + (idx & 3), p = (xcd >> 1) & 1, n0 = blk * 128;
  Loader L;
  u32x4 hx[8];
  {
    Order ord(cls);
    const int k0 = ord(0), k1 = ord(1), k2 = ord(2);
    if (idx >= 16) {
      L3.init(a, c, xcd, idx);
      L3.W(0, k0);
      L3.W(ST3, k1);
      LWAIT(14);
      signal(c, E_CL);
      if (w == 0) {
        await<2, 1>(E_CC);
        set_flag(c, F_C + xcd * 16 + idx - 16);
      }
    }
    L.rA = c.ws;
    L.rW = mk_rsrc(a.w4, F * D * 2);
    L.voA = lane * 16;
    L.voW = (4 * ((lane >> 4) & 1) + 16 * (lane >> 5)) * (D * 2) + (lane & 15) * 16;
    L.sa0 = WS_G3 + (w & 1) * 65536 + (m0 + p * 64 + (w >> 1) * 32) * 32;
    L.sa1 = L.sa0 ^ 2048;
    L.sw = w * (D * 2) + n0 * 2;
    L.la = (w & 1) * 4096 + (w >> 1) * 2048;
    L.lw = AB + w * 1056;
    {
      const int e = lane < 2 * NFS ? lane : 2 * NFS + ((lane - 2 * NFS) >> 1), pp = (lane >= 2 * NFS && (lane & 1)) ? p ^ 1 : p;
      const int m = e - 21, q = m / 5, r = (m + cls) % 5;
      const int k = e < 7 ? blk * 7 + e : e < 21 ? ((blk + 1 + ((e - 7) >> 1)) & 7) * 7 + 3 * ((e - 7) & 1) : (q + (q >= blk)) * 7 + r + 1 + (r >= 2);
      L.tab = pp * 112 + 2 * min(k, 55);
    }
    const int r16 = lane & 15, g = lane >> 4, row0 = m0 + p * 64 + (w >> 1) * 32;
    const int tl = min(w * 64 + lane, 223), q = tl >> 3, cc = q % 14;
    u32 o1 = (16 * (g & 1) + r16) * 32 + (g >> 1) * 16, ox = r16 * (D * 2) + 32 * (g & 1) + 16 * (g >> 1);
    u32 os = g * 4096 + r16 * 16, oc = ((cc < 12 ? (q / 14) * 12 + cc : 12 + 2 * (q / 14) + cc) * 8 + (tl & 7)) * 4;
    u32 oy = (lane >> 3) * (S * 2) + (((lane & 7) ^ (lane >> 3 & 6)) * 16);
    u32 fo = w ? OOB
             : lane < 8  ? foff(F_ST + tm * 8 + lane)
             : lane < 24 ? foff(F_C + blk * 16 + lane - 8)
             : lane < 36 ? foff(F_C + ((xcd & 3) + 4 * ((lane >> 1) & 1)) * 16 + 2 * min(((7 * (idx & 3)) >> 2) + ((lane - 24) >> 2), 6) + (lane & 1))
                         : OOB;
    asm volatile("" : "+v"(o1), "+v"(ox), "+v"(os), "+v"(oc), "+v"(oy), "+v"(fo), "+v"(L.tab), "+v"(L.voW));
    if (w == 0) {
      poll(c, lane < 8 ? foff(F_X1 + tm * 8 + lane) : OOB);
      signal_all(E_XGO);
    } else {
      await<1, 1>(E_XGO);
    }
    L3.A(0, k0);
    L3.A(ST3, k1);
    L3.W(2 * ST3, k2);
    L3.A(2 * ST3, k2);
    LWAIT(15);
    LBAR();
    set_flag(c, F_ST + ((xcd >> 1) * 4 + (idx >> 2)) * 8 + blk, w == 0 && idx < 16);
    LWAIT(12);
    LBAR();
    u32 lb = 0;
    auto issue = [&](int u) {
      const int kt = ord(u);
      L3.A(lb, kt);
      L3.W(lb, kt);
      lb = (lb == 2 * ST3) ? 0 : lb + ST3;
    };
    const rsrc_t rX = mk_rsrc(a.x, S * D * 2);
    const u32 s1o = WS_X1 + (blk * 8 + (w & 1) * 4) * 65536 + row0 * 32, sxo = row0 * (D * 2) + (n0 + (w & 1) * 64) * 2;
    const u32 syo = WS_YT + (n0 + w * 32) * (S * 2) + (m0 + p * 64) * 2, sco = WS_C3 + ((xcd & 3) * 4 + (idx & 3)) * 896;
    issue(3);
    LWAIT(12);
    LBAR();
#pragma unroll
    for (int k = 2; k <= 5; ++k) {
#pragma unroll
      for (int i = 2 * k - 4; i < 2 * k - 2; ++i)
        hx[i] = i < 4 ? ald16<true>(c.ws, o1, s1o + i * 65536) : ald16<false>(rX, ox, sxo + ((i >> 1) & 1) * (16 * D * 2) + (i & 1) * 64);
      issue(k + 2);
      LWAIT(13);
      LBAR();
    }
    u32x4 xg = ald16<true>(c.ws, L.voA, WS_XS + (m0 + p * 64) * 16);
    issue(8);
    LWAIT(12);
    LBAR();
    const u32x2 fv = peek(c, fo);
    issue(9);
    LWAIT(12);
    LBAR();
    issue(10);
    TRA();
    check<11>(c, fv, fo);
    LBAR();
    u32x4 sv[4];
    u32 cv[4];
#pragma unroll
    for (int k = 9; k <= 12; ++k) {
      const int i = k - 9;
      sv[i] = ald16<true>(c.ws, os, WS_ST + (tm * 16 + i) * 1024 + w * 256);
      cv[i] = ald4(c.ws, oc, sco + i * (F * 4));
      glds16<true>(c.ws, L_TB + w * 4096 + i * 1024, oy, syo + i * (8 * S * 2));
      issue(k + 2);
      LWAIT(14);
      LBAR();
    }
    issue(15);
    LWAIT(11);
    LBAR();
    L.seek(1);
    L.issue<1>();
#pragma unroll
    for (int i = 0; i < 4; ++i) {
      LANDED(sv[i]);
      LANDED(cv[i]);
    }
    const f32x4 t = (__builtin_bit_cast(f32x4, sv[0]) + __builtin_bit_cast(f32x4, sv[1])) + (__builtin_bit_cast(f32x4, sv[2]) + __builtin_bit_cast(f32x4, sv[3]));
    float s1 = red32(t[0], t[2]), s2 = red32(t[1], t[3]);
    s1 = red16(s1, s1);
    s2 = red16(s2, s2);
    const float mean = s1 * (1.0f / D);
    const float var = fmaxf(s2 * (1.0f / D) - mean * mean, 0.f);
    LDS(f32x4, L_T3 + (32 * w + 2 * (lane & 15) + (lane >> 5)) * 16) = gelu_par(-rbf(mean), rbf(__builtin_amdgcn_sqrtf(rbf(rbf(var) + 1e-5f))));
    LDS(float, L_T3 + 2048 + tl * 4) = (__uint_as_float(cv[0]) + __uint_as_float(cv[1])) + (__uint_as_float(cv[2]) + __uint_as_float(cv[3]));
    PIN();
    LWAIT(4);
    LBAR();
    L.issue<1>();
    L.issue<1>();
    LANDED(xg);
    LDS(f32x2, L_DUM + lane * 8) = lo2(ln_par(__uint_as_float(xg[0]), __uint_as_float(xg[2])));
    WAIT_LGKM0();
    LBAR();
  }
  L.seek(0);
  L.issue<1>();
  LWAIT(0);
  signal(c, E_4L);
  if (w == 0) {
    await<1, 1>(E_GE);
    set_flag(c, F_GE + tm * 16 + p * 8 + blk);
    poll(c, lane < 8 ? foff(F_GE + tm * 16 + p * 8 + lane) : OOB);
    signal_all(E_GGO);
  } else {
    await<1, 1>(E_GGO);
  }
  L.seek(3);
  L.issue<4>();
  await<1, 1>(E_4S0);
  L.issue<7>();
  await<1, 0>(E_4S1);
  L.issue<7>();
  LWAIT(16);
  LBAR();
  set_flag(c, F_GL + tm * 16 + p * 8 + blk, w == 0);
  L.issue<7>();
  LWAIT(17);
  LBAR();
  L.issue<7>();
  LWAIT(16);
  LBAR();
#pragma unroll 1
  for (int h = 0; h < 2; ++h) {
    const u32 fo = (w == 0 && lane < 8) ? foff(F_GL + tm * 16 + (p ^ h) * 8 + lane) : OOB;
    const u32x2 fv = peek(c, fo);
    L.issue<7>();
    LWAIT(17);
    LBAR();
    L.issue<7>();
    TRA();
    check<16>(c, fv, fo);
    LBAR();
    if (h) break;
#pragma unroll 1
    for (int k = 7; k <= NFS - 6; ++k) {
      L.issue<7>();
      LWAIT(16);
      LBAR();
    }
  }
#pragma unroll
  for (int k = 0; k < 4; ++k) {
    LANDED(hx[2 * k]);
    LANDED(hx[2 * k + 1]);
    LDS(u32x4, L.lb + L.la + 1024 + lane * 16) = hx[2 * k];
    LDS(u32x4, L.lb + L.la + 1024 + 8192 + lane * 16) = hx[2 * k + 1];
    L.issue<15>();
    WAIT_LGKM0();
    LWAIT(k == 0 ? 14 : 12);
    LBAR();
  }
  const u32 fo = (w == 0 && lane == 0) ? foff(F_EX + (blockIdx.x ^ 2)) : OOB;
  u32x2 fv;
#pragma unroll
  for (int k = 1; k < TAIL - 1; ++k) {
    if (k == TA + 1) set_flag(c, F_EX + blockIdx.x, w == 0);
    if (k == TB - 1) fv = peek(c, fo);
    if (k + 3 < TAIL) L.issue<15>();
    const int n = (k + 2 < TAIL ? 6 : 0) + (k + 3 < TAIL ? 6 : 0);
    if (k == TB) {
      TRA();
      check<(TB + 2 < TAIL ? 6 : 0) + (TB + 3 < TAIL ? 6 : 0)>(c, fv, fo);
    } else {
      LWAIT(n + (k == TA + 1 || k == TA + 2) + (k == TB - 1));
    }
    LBAR();
  }
  if (blockIdx.x == 0 && w == 0) {
#pragma unroll 1
    for (int i = 0; i < 4; ++i) poll(c, foff(F_GL + i * 64 + lane));
    if (lane == 0) __hip_atomic_store((gu64*)a.ws, c.tag, __ATOMIC_RELAXED, __HIP_MEMORY_SCOPE_AGENT);
  }
  TR_OUT(a.ws, 1);
  asm volatile(".Lfused_last:");
}

__device__ __forceinline__ void cons34(const Args& a, const Ctx& c, int xcd, int idx) {
  const int lane = fresh(c.lane), w = c.w;
  const int tm = (xcd >> 2) * 8 + (idx >> 2), m0 = tm * 128;
  const int wm = w >> 1, wn = w & 1, r16 = lane & 15, g = lane >> 4;
  const int blk = (xcd & 1) * 4 + (idx & 3), p = (xcd >> 1) & 1, n0 = blk * 128;
  const int row0 = m0 + p * 64 + wm * 32;
  const u32 vo = (16 * (g & 1) + r16) * 32 + (g >> 1) * 16;
  const u32 ab0 = (g >> 1) * 4096 + (wm * 64 + r16) * 32 + (g & 1) * 16;
  {
    STAMP_DECL; STAMP(0);
    const int tn = (xcd & 3) * 4 + (idx & 3);
    f32x4 acc[4][7];
    Frag<7> f0, f1;
    u32 ab = ab0, wb = AB + wn * 224 + 8 * (lane & 3) + (g & 1) * 7200 + ((r16 >> 2) + 8 * (g >> 1)) * 448;
#pragma unroll
    for (int i = 0; i < 4; ++i)
#pragma unroll
      for (int j = 0; j < 7; ++j) {
        acc[i][j] = f32x4{0.f, 0.f, 0.f, 0.f};
        asm volatile("" : "+v"(acc[i][j]));
      }
    WAIT_VM(0);
    signal(c, E_CC);
    BAR();
    STAMP(1);
#pragma unroll
    for (int r = 0; r < 18; ++r) rd<224, 4, 512>(f0, r, ab, wb);
    WAIT_LGKM0();
    PIN();
    int st = 0;
#pragma unroll 1
    for (int kt = 0; kt < 15; ++kt) step<224, 4, 512, 8192, ST3, 3, 0>(acc, f0, f1, ab, wb, st);
    step<224, 4, 512, 8192, ST3, 3, 2>(acc, f0, f1, ab, wb, st);
    STAMP(2);
    f32x4 q[4];
#pragma unroll
    for (int i = 0; i < 4; ++i) q[i] = LDS(f32x4, L_T3 + (wm * 64 + 16 * i + r16) * 16);
#pragma unroll
    for (int j = 0; j < 7; ++j) {
      const f32x4 cs = LDS(f32x4, L_T3 + 2048 + (wn * 112 + 16 * j + 4 * g) * 4);
      const int kg = j < 6 ? wn * 6 + j : 12 + wn;
#pragma unroll
      for (int ip = 0; ip < 2; ++ip) {
        const u32x4 v = gelu16(acc[2 * ip][j], acc[2 * ip + 1][j], cs, q[2 * ip], q[2 * ip + 1]);
        st16(c.ws, vo, WS_G3 + (tn * 14 + kg) * 65536 + (m0 + wm * 64 + ip * 32) * 32, v);
        LDS(u32x4, (kg >> 2) * ST2 + (kg & 3) * 4096 + (2 * ip + (wm ^ p)) * 1024 + vo) = v;
      }
      if (j == GEJ) {
        WAIT_VM(2 * GEJ - 2);
        signal(c, E_GE);
      }
    }
    STAMP(3);
    STAMP_OUT(a.ws, 4);
  }
  STAMP_DECL; STAMP(0);
  signal(c, E_4C);
  f32x4 acc[4][4];
  Frag<4> f0, f1;
#pragma unroll
  for (int i = 0; i < 4; ++i)
#pragma unroll
    for (int j = 0; j < 4; ++j) {
      acc[i][j] = f32x4{0.f, 0.f, 0.f, 0.f};
      asm volatile("" : "+v"(acc[i][j]));
    }
  u32 ab = ab0, wb = AB + wn * 128 + 8 * (lane & 3) + ((r16 >> 2) + 4 * (g & 1)) * 1056 + (g >> 1) * 512;
  await<2, 0>(E_4C);
  STAMP(1);
#pragma unroll
  for (int r = 0; r < 12; ++r) rd<128, 4, 512>(f0, r, ab, wb);
  WAIT_LGKM0();
  PIN();
  int st = 0;
#pragma unroll 1
  for (int kt = 0; kt < 2; ++kt) step<128, 4, 512, 8192, ST2, 4, 1>(acc, f0, f1, ab, wb, st, L_CNT + 16 * (E_4S0 + kt) + 4 * w);
#pragma unroll 1
  for (int kt = 2; kt < NFS; ++kt) step<128, 4, 512, 8192, ST2, 4, 4>(acc, f0, f1, ab, wb, st);
#pragma unroll
  for (int j = 0; j < 4; ++j)
    st16(c.ws, lane * 16, WS_EX + (blockIdx.x * 4 + w) * 4096 + j * 1024,
         u32x4{pack2(acc[2][j][0], acc[2][j][1]), pack2(acc[2][j][2], acc[2][j][3]), pack2(acc[3][j][0], acc[3][j][1]), pack2(acc[3][j][2], acc[3][j][3])});
  PIN();
  const u32 hb = (w & 1) * 4096 + (w >> 1) * 2048 + 1024 + lane * 16;
  f32x4 sv[2][4];
  u32x4 xv[2][2], ex[4];
  auto prep = [&](int k) {
    if (k == 1 || k == 2) {
      const int i = k - 1;
      const f32x2 xs = LDS(f32x2, L_DUM + (wm * 32 + 16 * i + r16) * 8);
#pragma unroll
      for (int j = 0; j < 4; ++j) {
        if (!(j & 1)) {
          const u32x4 xr = LDS(u32x4, ((NFS + 2 + i) & 3) * ST2 + (j >> 1) * 8192 + hb);
          xv[i][j / 2] = swap16(xr[0], xr[1], xr[2], xr[3]);
        }
        const int dr = wn * 64 + 16 * j + 4 * g + (r16 >> 2);
        const u32x2 yv = __builtin_bit_cast(u32x2, lds_tr(L_TB + dr * 128 + (((2 * wm + i) ^ (dr >> 1 & 3)) * 32) + 8 * (lane & 3)));
        const f32x2 l = fma2(bf_2(xv[i][j / 2][2 * (j & 1)]), dup2(xs[0]), dup2(xs[1])) * bf_2(yv[0]);
        const f32x2 u = fma2(bf_2(xv[i][j / 2][2 * (j & 1) + 1]), dup2(xs[0]), dup2(xs[1])) * bf_2(yv[1]);
        sv[i][j] = f32x4{l[0], l[1], u[0], u[1]};
        asm volatile("" : "+v"(sv[i][j]));
      }
    }
    if (k == 3) {
#pragma unroll
      for (int j = 0; j < 4; ++j) {
        const u32x4 xr = LDS(u32x4, ((NFS + (j >> 1)) & 3) * ST2 + (j & 1) * 8192 + hb);
        const u32x4 x1 = swap16(xr[0], xr[1], xr[2], xr[3]);
#pragma unroll
        for (int i = 0; i < 2; ++i) {
          acc[2 + i][j] = f32x4{bf_lo(x1[2 * i]), bf_hi(x1[2 * i]), bf_lo(x1[2 * i + 1]), bf_hi(x1[2 * i + 1])};
          asm volatile("" : "+v"(acc[2 + i][j]));
        }
      }
    }
    if (k == TB + 1) {
#pragma unroll
      for (int j = 0; j < 4; ++j) ex[j] = ld16(c.ws, lane * 16, WS_EX + ((blockIdx.x ^ 2) * 4 + w) * 4096 + j * 1024);
    }
    PIN();
  };
#pragma unroll
  for (int k = 0; k < TAIL - 1; ++k) {
    prep(k);
    if (k == TA) step<128, 2, 512, 8192, ST2, 4, 4>(acc, f0, f1, ab, wb, st);
    else step<128, 2, 512, 8192, ST2, 4, 0>(acc, f0, f1, ab, wb, st);
  }
  prep(TAIL - 1);
  step<128, 2, 512, 8192, ST2, 4, 3>(acc, f0, f1, ab, wb, st);
  STAMP(2);
#pragma unroll
  for (int i = 0; i < 2; ++i)
#pragma unroll
    for (int jp = 0; jp < 2; ++jp) {
      u32 o[4];
#pragma unroll
      for (int e = 0; e < 4; ++e) {
        const int j = 2 * jp + (e >> 1);
        const f32x4 t = acc[i][j] + acc[2 + i][j];
        const f32x2 ob = ((e & 1) ? hi2(t) : lo2(t)) + bf_2(ex[j][2 * i + (e & 1)]);
        const f32x2 z = ob * dup2(-1.442695041f);
        const f32x2 d = f32x2{__builtin_amdgcn_exp2f(z[0]), __builtin_amdgcn_exp2f(z[1])} + dup2(1.0f);
        const f32x2 gt = ob * f32x2{__builtin_amdgcn_rcpf(d[0]), __builtin_amdgcn_rcpf(d[1])};
        const f32x2 r = fma2((e & 1) ? hi2(sv[i][j]) : lo2(sv[i][j]), gt, bf_2(xv[i][jp][e]));
        o[e] = pack2(r[0], r[1]);
      }
      *(u32x4*)(a.out + (size_t)(row0 + 16 * i + r16) * D + n0 + wn * 64 + 32 * jp + 16 * (g & 1) + 8 * (g >> 1)) = swap16(o[0], o[1], o[2], o[3]);
    }
  STAMP(3);
  STAMP_OUT(a.ws, 5);
}

__global__ __launch_bounds__(512, 2) void fused(Args a) {
  const int xcd = blockIdx.x & 7, idx = blockIdx.x >> 3;
  const int w8 = __builtin_amdgcn_readfirstlane(threadIdx.x >> 6);
  Ctx c;
  c.tag = *(const __attribute__((address_space(4))) u64*)a.ws;
  c.ws = mk_rsrc(a.ws, WS_END);
  c.lane = threadIdx.x & 63;
  c.w = w8 & 3;
  Loader3 L3;
  if (idx >= 16 && w8 == 0) {
    u64 pc;
    u32 len;
    asm volatile("s_getpc_b64 %0\n.Lfused_first:\n\ts_mov_b32 %1, .Lfused_last-.Lfused_first" : "=s"(pc), "=s"(len));
    touch(mk_rsrc((const void*)pc, len), L_DUM, c.lane < 40 ? c.lane * 128 + (idx - 16) * 5120 : OOB, 0);
  }
  if (__builtin_expect(idx < 16, 1)) {
    if (__builtin_expect(w8 >= 4, 1)) load12(a, c, xcd, idx, L3);
    else cons12(a, c, xcd, idx);
  } else {
    if (w8 >= 4) side_kv(a, c, xcd, idx - 16);
    else side_stats(a, c, xcd, idx - 16);
    conv(a, c, w8, xcd, idx - 16);
  }
  if (w8 >= 4) load34(a, c, xcd, idx, L3);
  else cons34(a, c, xcd, idx);
}

extern "C" void kernel_launch(void* const* d_in, const int* in_sizes, int n_in, void* d_out, int out_size, void* d_ws,
                              size_t ws_size, hipStream_t stream) {
  Args a;
  a.x = (const u16*)d_in[0];
  a.k = (const u16*)d_in[1];
  a.v = (const u16*)d_in[2];
  a.shk = (const u16*)d_in[3];
  a.ssm_k = (const u16*)d_in[4];
  a.ssm_D = (const u16*)d_in[5];
  a.w1 = (const u16*)d_in[7];
  a.w2 = (const u16*)d_in[8];
  a.w3 = (const u16*)d_in[9];
  a.w4 = (const u16*)d_in[10];
  a.ws = (char*)d_ws;
  a.out = (u16*)d_out;
  (void)hipFuncSetAttribute((const void*)fused, hipFuncAttributeMaxDynamicSharedMemorySize, LDS_BYTES);
  fused<<<256, 512, LDS_BYTES, stream>>>(a);
}
```

```cpp
#define KE 1
#define KV_SLEEP 28
#include <hip/hip_runtime.h>
#include <hip/hip_bf16.h>
#include <stdint.h>
#pragma clang diagnostic ignored "-Wint-to-pointer-cast"

typedef unsigned short u16;
typedef unsigned int u32;
typedef unsigned long long u64;
typedef __attribute__((ext_vector_type(8))) __bf16 bf16x8;
typedef __attribute__((ext_vector_type(4))) short s16x4;
typedef __attribute__((ext_vector_type(4))) float f32x4;
typedef __attribute__((ext_vector_type(2))) float f32x2;
typedef __attribute__((ext_vector_type(2))) u32 u32x2;
typedef __attribute__((ext_vector_type(4))) u32 u32x4;
typedef __attribute__((ext_vector_type(2))) __bf16 bf16x2;
typedef __attribute__((address_space(1))) u64 gu64;
typedef __amdgpu_buffer_rsrc_t rsrc_t;
#define AS3 __attribute__((address_space(3)))
#define LDS(T, off) (*(AS3 T*)(u32)(off))

constexpr int S = 2048, D = 1024, F = 3584;

constexpr u32 WS_FL = 1024;
constexpr u32 F_KV = 0, F_C = 128, F_HE = 256, F_HL = 384, F_X1 = 512, F_ST = 640, F_GE = 768, F_GL = 1024, F_EX = 1280;
constexpr u32 WS_XS = WS_FL + 1536 * 128;
constexpr u32 WS_C1 = WS_XS + S * 16;
constexpr u32 WS_C3 = WS_C1 + 32 * 1024;
constexpr u32 WS_ST = WS_C3 + 4 * F * 4;
constexpr u32 WS_G3 = 1u << 20;
constexpr u32 WS_H = WS_G3;
constexpr u32 WS_UR = WS_H + S * D * 2;
constexpr u32 WS_X1 = WS_G3 + S * F * 2;
constexpr u32 WS_END = WS_X1 + S * D * 2;
static_assert(WS_ST + 16 * 16 * 128 * 8 <= WS_H, "ws");

constexpr int CV_H = 4576, CV_U = 4192, CV_CH = 2 * CV_H + 2 * CV_U;
constexpr u32 L_TB = 8 * CV_CH;
constexpr u32 L_T3 = L_TB + 16384;
constexpr u32 L_DUM = L_T3 + 3072;
constexpr u32 L_CNT = L_DUM + 2048;
enum { E_SUM, E_KI, E_KV, E_2C, E_2L, E_HE, E_HGO, E_2S, E_X1, E_XGO, E_4C, E_4L, E_GE, E_GGO, E_4S0, E_4S1, E_N };
constexpr u32 L_LN = L_CNT + 512;
constexpr int LDS_BYTES = 163840;
static_assert(L_LN + 512 <= LDS_BYTES && E_N <= 32, "lds");
constexpr u32 OOB = 0x80000000u;

__device__ __forceinline__ float bf_lo(u32 v) { return __uint_as_float(v << 16); }
__device__ __forceinline__ float bf_hi(u32 v) { return __uint_as_float(v & 0xffff0000u); }
__device__ __forceinline__ f32x2 bf_2(u32 v) { return f32x2{bf_lo(v), bf_hi(v)}; }
__device__ __forceinline__ u32 pack2(float lo, float hi) {
  bf16x2 r;
  r[0] = (__bf16)lo;
  r[1] = (__bf16)hi;
  return __builtin_bit_cast(u32, r);
}
__device__ __forceinline__ float rbf(float x) { return (float)(__bf16)x; }
__device__ __forceinline__ f32x2 fma2(f32x2 a, f32x2 b, f32x2 c) { return __builtin_elementwise_fma(a, b, c); }
__device__ __forceinline__ f32x2 dup2(float a) { return f32x2{a, a}; }
__device__ __forceinline__ f32x2 lo2(f32x4 v) { return f32x2{v[0], v[1]}; }
__device__ __forceinline__ f32x2 hi2(f32x4 v) { return f32x2{v[2], v[3]}; }
__device__ __forceinline__ float dot2(u32 a, u32 b, float c) {
  return __builtin_amdgcn_fdot2_f32_bf16(__builtin_bit_cast(bf16x2, a), __builtin_bit_cast(bf16x2, b), c, false);
}
__device__ __forceinline__ u32 sconst(u32 v) {
  asm("" : "+s"(v));
  return v;
}
template <int CTRL>
__device__ __forceinline__ float dpp_add(float v) {
  return v + __int_as_float(__builtin_amdgcn_update_dpp(0, __float_as_int(v), CTRL, 0xf, 0xf, true));
}
__device__ __forceinline__ float red32(float a, float b) {
  const u32x2 s = __builtin_amdgcn_permlane32_swap(__float_as_uint(a), __float_as_uint(b), false, false);
  return __uint_as_float(s[0]) + __uint_as_float(s[1]);
}
__device__ __forceinline__ float red16(float a, float b) {
  const u32x2 s = __builtin_amdgcn_permlane16_swap(__float_as_uint(a), __float_as_uint(b), false, false);
  return __uint_as_float(s[0]) + __uint_as_float(s[1]);
}
__device__ __forceinline__ u32x4 swap16(u32 x01, u32 x23, u32 y01, u32 y23) {
  const u32x2 s0 = __builtin_amdgcn_permlane16_swap(x01, y01, false, false);
  const u32x2 s1 = __builtin_amdgcn_permlane16_swap(x23, y23, false, false);
  return u32x4{s0[0], s1[0], s0[1], s1[1]};
}

#define STAMP(i)
#define STAMP_DECL
#define STAMP_OUT(ws, kid)
#define WAIT_VM(n) asm volatile("s_waitcnt vmcnt(%0)" ::"n"(n) : "memory")
#define WAIT_LGKM0() __builtin_amdgcn_s_waitcnt(0xC07F)
#define PIN() __builtin_amdgcn_sched_barrier(0)
#define BAR() __builtin_amdgcn_s_barrier()
#define TR_DECL
#define TRA()
#define LWAIT(n) WAIT_VM(n)
#define LBAR() BAR()
#define TR_OUT(ws, ph)

__device__ __forceinline__ rsrc_t mk_rsrc(const void* p, u32 bytes) {
  return __builtin_amdgcn_make_buffer_rsrc((void*)p, 0, bytes, 0x00020000);
}
template <bool SC1>
__device__ __forceinline__ void glds16(rsrc_t r, u32 lds_off, u32 voff, u32 soff) {
  if (SC1)
    asm volatile("s_mov_b32 m0, %0\n\ts_nop 0\n\tbuffer_load_dwordx4 %1, %2, %3 offen sc1 lds" ::"s"(lds_off), "v"(voff), "s"(r), "s"(soff) : "memory");
  else
    asm volatile("s_mov_b32 m0, %0\n\ts_nop 0\n\tbuffer_load_dwordx4 %1, %2, %3 offen lds" ::"s"(lds_off), "v"(voff), "s"(r), "s"(soff) : "memory");
}
__device__ __forceinline__ void touch(rsrc_t r, u32 lds_off, u32 voff, u32 soff) {
  asm volatile("s_mov_b32 m0, %0\n\ts_nop 0\n\tbuffer_load_dword %1, %2, %3 offen lds" ::"s"(lds_off), "v"(voff), "s"(r), "s"(soff) : "memory");
}
__device__ __forceinline__ s16x4 lds_tr(u32 off) { return __builtin_amdgcn_ds_read_tr16_b64_v4i16((AS3 s16x4*)off); }
__device__ __forceinline__ void st16(rsrc_t r, u32 voff, u32 soff, u32x4 v) { __builtin_amdgcn_raw_buffer_store_b128(v, r, voff + soff, 0, 16); }
__device__ __forceinline__ u32x4 ld16(rsrc_t r, u32 voff, u32 soff) { return __builtin_amdgcn_raw_buffer_load_b128(r, voff, soff, 16); }

struct Args {
  const u16 *x, *k, *v, *shk, *ssm_k, *ssm_D, *w1, *w2, *w3, *w4;
  char* ws;
  u16* out;
};

struct Ctx {
  rsrc_t ws;
  u64 tag;
  int lane, w;
};
__device__ __forceinline__ int fresh(int v) {
  asm volatile("" : "+v"(v));
  return v;
}
__device__ __forceinline__ u32 foff(u32 f) { return WS_FL + f * 128; }
__device__ __forceinline__ void set_flag(const Ctx& c, u32 f, bool on = true) {
  const u32x2 t = {(u32)c.tag, (u32)(c.tag >> 32)};
  asm volatile("buffer_store_dwordx2 %0, %1, %2, %3 offen sc1" ::"v"(t), "v"((c.lane == 0 && on) ? 0u : OOB), "s"(c.ws), "s"(foff(f)) : "memory");
}
__device__ __forceinline__ u32x4 tagged(const Ctx& c, float x, float y) {
  return u32x4{__float_as_uint(x), (u32)c.tag, __float_as_uint(y), (u32)c.tag};
}
__device__ __forceinline__ bool stale(const Ctx& c, u32x4 v) { return v[1] != (u32)c.tag || v[3] != (u32)c.tag; }
template <bool SC1>
__device__ __forceinline__ u32x4 ald16(rsrc_t r, u32 voff, u32 soff) {
  u32x4 v;
  if (SC1) asm volatile("buffer_load_dwordx4 %0, %1, %2, %3 offen sc1" : "=v"(v) : "v"(voff), "s"(r), "s"(soff) : "memory");
  else asm volatile("buffer_load_dwordx4 %0, %1, %2, %3 offen" : "=v"(v) : "v"(voff), "s"(r), "s"(soff) : "memory");
  return v;
}
__device__ __forceinline__ u32 ald4(rsrc_t r, u32 voff, u32 soff) {
  u32 v;
  asm volatile("buffer_load_dword %0, %1, %2, %3 offen sc1" : "=v"(v) : "v"(voff), "s"(r), "s"(soff) : "memory");
  return v;
}
#define LANDED(v) asm volatile("" : "+v"(v))
__device__ __forceinline__ f32x4 ln_par(float mean, float var) {
  const float m1 = rbf(mean), sd1 = rbf(__builtin_amdgcn_sqrtf(rbf(rbf(var) + 1e-5f)));
  const float rs1 = __builtin_amdgcn_rcpf(sd1);
  const float m2 = rbf((mean - m1) * rs1), sd2 = rbf(__builtin_amdgcn_sqrtf(rbf(rbf(var * rs1 * rs1) + 1e-5f)));
  return f32x4{rs1, -m1 * rs1, -__builtin_fmaf(m2, sd1, m1), sd1 * sd2};
}
#define PSL 4
__device__ __forceinline__ u32x2 peek(const Ctx& c, u32 off) {
  u32x2 v;
  asm volatile("buffer_load_dwordx2 %0, %1, %2, 0 offen sc1" : "=v"(v) : "v"(off), "s"(c.ws) : "memory");
  return v;
}
template <int N>
__device__ __forceinline__ void check(const Ctx& c, u32x2 v, u32 off) {
  asm volatile("s_waitcnt vmcnt(%1)" : "+v"(v) : "n"(N) : "memory");
#pragma unroll 1
  for (int n = 0; n < (1 << 20) && __builtin_amdgcn_ballot_w64(__builtin_bit_cast(u64, v) != c.tag && off != OOB) != 0; ++n) {
    __builtin_amdgcn_s_sleep(PSL);
    v = peek(c, off);
    asm volatile("s_waitcnt vmcnt(0)" : "+v"(v)::"memory");
  }
}
__device__ __forceinline__ void poll(const Ctx& c, u32 off) { check<0>(c, peek(c, off), off); }
__device__ __forceinline__ u32 arrive(const Ctx& c, u32 e) {
  u32 old = 0;
  if (c.lane == 0) old = __hip_atomic_fetch_add((AS3 u32*)(L_CNT + 16 * e), 1u, __ATOMIC_RELAXED, __HIP_MEMORY_SCOPE_WORKGROUP);
  return __builtin_amdgcn_readfirstlane(old);
}
__device__ __forceinline__ void signal(const Ctx& c, u32 e) {
  asm volatile("" ::: "memory");
  LDS(volatile u32, L_CNT + 16 * e + 4 * c.w) = 1u;
}
__device__ __forceinline__ void signal_all(u32 e) {
  asm volatile("" ::: "memory");
  LDS(volatile u32x4, L_CNT + 16 * e) = u32x4{1u, 1u, 1u, 1u};
}
template <int N, int SLEEP>
__device__ __forceinline__ void await(u32 e) {
#pragma unroll 1
  for (int n = 0; n < (1 << 24); ++n) {
    u32x4 v = LDS(volatile u32x4, L_CNT + 16 * e);
    if (N == 2) v &= LDS(volatile u32x4, L_CNT + 16 * e + 16);
    if (__builtin_amdgcn_readfirstlane(v[0] & v[1] & v[2] & v[3])) break;
    if (SLEEP) __builtin_amdgcn_s_sleep(SLEEP);
  }
  asm volatile("" ::: "memory");
}
#define TAG_WAIT(c) do { PIN(); (c).tag += 1; asm volatile("s_waitcnt lgkmcnt(0)" : "+s"((c).tag) :: "memory"); } while (0)

#define TAIL 8
constexpr int NFS = 28 - TAIL / 2;

struct Pre {
  u32 voW[7], os, oc, oy, fo, voW4;
  int tab;
  static __device__ __forceinline__ u32 adj(u32 q) {
    return 12 * __builtin_amdgcn_ubfe(sconst(0x3000u), q, 1) - 2 * __builtin_amdgcn_ubfe(sconst(0x3ffc000u), q, 1);
  }
  __device__ __forceinline__ void init(const Ctx& c, int xcd, int idx) {
    const int lane = fresh(c.lane) & 63, w = c.w;
    const int tm = (xcd >> 2) * 8 + (idx >> 2), cls = ((idx >> 2) + idx) & 3, blk = (xcd & 1) * 4 + (idx & 3), p = (xcd >> 1) & 1;
#pragma unroll
    for (int i = 0; i < 7; ++i) {
      const u32 l = i * 64 + lane, r = (l * 2341) >> 16;
      voW[i] = (l + r * 420 + (i > 3 || (i == 3 && lane >= 32) ? 8 * 448 : 0) + adj(l - r * 28)) * 16;
      asm volatile("" : "+v"(voW[i]));
    }
    voW4 = (4 * ((lane >> 4) & 1) + 16 * (lane >> 5)) * (D * 2) + (lane & 15) * 16;
    {
      const int e = lane < 2 * NFS ? lane : 2 * NFS + ((lane - 2 * NFS) >> 1), pp = (lane >= 2 * NFS && (lane & 1)) ? p ^ 1 : p;
      const int m = max(e - 21, 0), q = (m * 13) >> 6, r = m + cls - 5 * (((m + cls) * 13) >> 6);
      const int k = e < 7 ? blk * 7 + e : e < 21 ? ((blk + 1 + ((e - 7) >> 1)) & 7) * 7 + 3 * ((e - 7) & 1) : (q + (q >= blk)) * 7 + r + 1 + (r >= 2);
      tab = pp * 112 + 2 * min(k, 55);
    }
    const u32 tl = min(w * 64 + lane, 223);
    os = (lane >> 4) * 4096 + (lane & 15) * 16;
    oc = (tl + 8 * adj(tl >> 3)) * 4;
    oy = (lane >> 4) * (D * 2) + (lane & 8) * 16 + (((lane & 7) ^ (lane >> 3 & 6)) * 16);
    const int kg = ((xcd & 3) * 4 + (idx & 3)) * 14 + lane - 36;
    fo = w ? OOB
         : lane < 8  ? foff(F_ST + tm * 8 + lane)
         : lane < 24 ? foff(F_C + blk * 16 + lane - 8)
         : lane < 36 ? foff(F_C + ((xcd & 3) + 4 * ((lane >> 1) & 1)) * 16 + 2 * min(((7 * (idx & 3)) >> 2) + ((lane - 24) >> 2), 6) + (lane & 1))
         : (u32)(kg - 64) < 64u && lane < 50 ? foff(F_C + (kg - 64) * 2 + (tm >> 3))
                     : OOB;
    asm volatile("" : "+v"(os), "+v"(oc), "+v"(oy), "+v"(fo), "+v"(tab), "+v"(voW4));
  }
};
__device__ __forceinline__ void ln_rows(const Ctx& c, int xcd, int idx) {
  const u32 so = WS_XS + (((xcd >> 2) * 8 + (idx >> 2)) * 128 + ((xcd >> 1) & 1) * 64) * 16;
  u32x4 v;
#pragma unroll 1
  for (int n = 0; n < (1 << 20); ++n) {
    v = ald16<true>(c.ws, c.lane * 16, so);
    asm volatile("s_waitcnt vmcnt(0)" : "+v"(v)::"memory");
    if (__builtin_amdgcn_ballot_w64(stale(c, v)) == 0) break;
    __builtin_amdgcn_s_sleep(PSL);
  }
  LDS(f32x2, L_LN + c.lane * 8) = lo2(ln_par(__uint_as_float(v[0]), __uint_as_float(v[2])));
}

#define PFW 3
#define W2T 0
#define T3R 9
#define T4R (w8 % 3)
#define KV64
__device__ __forceinline__ void warm1(const Args& a, const Ctx& c, int xcd, int j16);
#define TP 0
#define TPL 4
#define TPX 0
__device__ __forceinline__ void touch_pages(const Args& a, const Ctx& c, int j16) {
  if (!TPX && c.w < 2) return;
  const int id = j16 + 16 * ((c.w + 2) & 3);
  const rsrc_t r = id < 4    ? mk_rsrc(a.ssm_k, D * S * 2)
                   : id < 8  ? mk_rsrc(a.out, S * D * 2)
                   : id < 16 ? mk_rsrc(a.w3, D * F * 2)
                   : id < 24 ? mk_rsrc(a.w4, F * D * 2)
                   : id < 28 ? mk_rsrc(a.w2, D * D * 2)
                   : id < 32 || !TPX ? mk_rsrc(a.ssm_D, D * 2)
                   : id < 36 ? mk_rsrc(a.x, S * D * 2)
                   : id < 40 ? mk_rsrc(a.w1, D * D * 2)
                             : c.ws;
  const u32 mb = TPX && id >= 40 ? id - 40 : id & (id >= 8 && id < 24 ? 7 : 3);
  const u32 off = c.lane < TPL ? (mb << 20) + c.lane * ((1 << 20) / TPL) : OOB;
  asm volatile("s_mov_b32 m0, %0\n\ts_nop 0\n\tbuffer_load_dword %1, %2, 0 offen sc1 lds" ::"s"(L_DUM + c.w * 256), "v"(off), "s"(r) : "memory");
}
__device__ __forceinline__ void side_stats(const Args& a, Ctx& c, int xcd, int j16) {
  STAMP_DECL; STAMP(0);
  const int lane = c.lane, w = c.w;
  const int row0 = xcd * 256 + j16 * 16 + 4 * w, col0 = (xcd & 1) * 512 + j16 * 32;
  const u16* xp = a.x + (size_t)(row0 + (lane >> 4)) * D + 8 * (lane & 15);
  const u16* wp = a.w1 + (size_t)((xcd >> 1) * 256 + 16 * w + (lane >> 2)) * D + col0 + 8 * (lane & 3);
  u32x4 xr[8], wr[4];
#pragma unroll
  for (int i = 0; i < 8; ++i) {
    xr[i] = *(const u32x4*)(xp + 128 * i);
    if (i < 4) wr[i] = *(const u32x4*)(wp + (size_t)64 * i * D);
  }
  PIN();
  auto warm2 = [&]() {
    if (w < 2)
      touch(mk_rsrc(a.w2, D * D * 2), L_DUM + w * 256, (lane >> 3) * 2048 + (lane & 7) * 128, ((xcd >> 1) * 256 + j16 * 16 + 8 * w) * 2048 + (xcd & 1) * 1024);
  };
  if (W2T == 2) warm2();
  if (TP == 0) touch_pages(a, c, j16);
  if (PFW == 2) warm1(a, c, xcd, j16);
  if (w == 0 && lane < E_N) LDS(u32x4, L_CNT + 16 * lane) = u32x4{0u, 0u, 0u, 0u};
  TAG_WAIT(c);
  BAR();
  PIN();
  const u32 o11 = sconst(0x3F803F80u), o01 = sconst(0x00003F80u), o10 = sconst(0x3F800000u);
  float s1 = 0.f, s2 = 0.f, cs[8];
#pragma unroll
  for (int e = 0; e < 8; ++e) cs[e] = 0.f;
#pragma unroll
  for (int i = 0; i < 8; ++i) {
#pragma unroll
    for (int e = 0; e < 4; ++e) {
      s1 = dot2(xr[i][e], o11, s1);
      s2 = dot2(xr[i][e], xr[i][e], s2);
      if (i < 4) {
        cs[2 * e] = dot2(wr[i][e], o01, cs[2 * e]);
        cs[2 * e + 1] = dot2(wr[i][e], o10, cs[2 * e + 1]);
      }
    }
    PIN();
  }
  STAMP(1);
  s1 = dpp_add<0x140>(dpp_add<0x141>(dpp_add<0x4E>(dpp_add<0xB1>(s1))));
  s2 = dpp_add<0x140>(dpp_add<0x141>(dpp_add<0x4E>(dpp_add<0xB1>(s2))));
  const float mean = s1 * (1.0f / D);
  const float var = fmaxf(s2 * (1.0f / D) - mean * mean, 0.f);
  st16(c.ws, (lane & 15) == 0 ? lane : 0x80000000u, WS_XS + row0 * 16, tagged(c, mean, var));
  const float u0 = red32(cs[0], cs[4]), u1 = red32(cs[1], cs[5]), u2 = red32(cs[2], cs[6]), u3 = red32(cs[3], cs[7]);
  const float t0 = dpp_add<0x128>(dpp_add<0x124>(red16(u0, u2))), t1 = dpp_add<0x128>(dpp_add<0x124>(red16(u1, u3)));
  if ((lane & 12) == 0) LDS(f32x2, L_T3 + w * 128 + ((lane & 3) * 4 + (lane >> 4)) * 8) = f32x2{t0, t1};
  asm volatile("" ::: "memory");
  if (arrive(c, E_SUM) == 3) {
    asm volatile("" ::: "memory");
    const u32 o = L_T3 + (lane & 15) * 8;
    const f32x2 t = (LDS(f32x2, o) + LDS(f32x2, o + 128)) + (LDS(f32x2, o + 256) + LDS(f32x2, o + 384));
    st16(c.ws, lane < 16 ? lane * 16 : 0x80000000u, WS_C1 + (((xcd & 1) * 16 + j16) * 4 + (xcd >> 1)) * 256, tagged(c, t[0], t[1]));
  }
  STAMP(2);
  if (PFW == 4) warm1(a, c, xcd, j16);
  if (W2T == 1) warm2();
  if (TP == 1) touch_pages(a, c, j16);
  await<1, 2>(E_KI);
  u32x4 hc[2][4];
#pragma unroll
  for (int q = 0; q < 2; ++q)
#pragma unroll
    for (int i = 0; i < 4; ++i) hc[q][i] = *(const u32x4*)(a.ssm_k + (size_t)((xcd * 16 + j16) * 8 + w + 4 * q) * S + 8 * (i * 64 + lane));
  PIN();
  if (W2T == 0) warm2();
#pragma unroll
  for (int q = 0; q < 2; ++q) {
    const u32 hb = (w + 4 * q) * CV_CH;
    if (lane < 56) LDS(u32x4, hb + (lane < 28 ? lane * 16 : CV_H + (lane - 28) * 16)) = u32x4{0u, 0u, 0u, 0u};
#pragma unroll
    for (int i = 0; i < 4; ++i) {
      u32x4 o;
      const u32 prev = __builtin_amdgcn_update_dpp(i ? __builtin_amdgcn_readlane(hc[q][i ? i - 1 : 0][3], 63) : 0u, hc[q][i][3], 0x138, 0xf, 0xf, false);
      o[0] = __builtin_amdgcn_alignbit(hc[q][i][0], prev, 16);
      o[1] = __builtin_amdgcn_alignbit(hc[q][i][1], hc[q][i][0], 16);
      o[2] = __builtin_amdgcn_alignbit(hc[q][i][2], hc[q][i][1], 16);
      o[3] = __builtin_amdgcn_alignbit(hc[q][i][3], hc[q][i][2], 16);
      LDS(u32x4, hb + 448 + (i * 64 + lane) * 16) = o;
      LDS(u32x4, hb + CV_H + 448 + (i * 64 + lane) * 16) = hc[q][i];
    }
  }
  STAMP(3);
  STAMP_OUT(a.ws, 0);
}

__device__ __forceinline__ void warm1(const Args& a, const Ctx& c, int xcd, int j16) {
  const int lane = c.lane, w = c.w;
#pragma unroll
  for (int i = 0; i < 2; ++i)
    touch(mk_rsrc(a.w1, D * D * 2), L_DUM + w * 256, (lane >> 3) * 2048 + (lane & 7) * 128, (j16 * 64 + w * 16 + 8 * i) * 2048 + (xcd & 1) * 1024);
  touch(mk_rsrc(a.x, S * D * 2), L_DUM + w * 256, lane * 128, ((xcd ^ 1) * 256 + j16 * 16 + 4 * w) * 2048);
}
__device__ __forceinline__ void side_kv(const Args& a, Ctx& c, int xcd, int j16, Pre& P) {
  STAMP_DECL; STAMP(0);
  const int lane = c.lane, w = c.w;
  if (PFW == 0) warm1(a, c, xcd, j16);
#define KT 1
  if (KT) {
    const rsrc_t r = w == 0 ? mk_rsrc(a.k, S * D * 2) : w == 1 ? mk_rsrc(a.v, S * D * 2) : w == 2 ? mk_rsrc(a.shk, D * 8) : mk_rsrc(a.ssm_D, D * 2);
    const u32 so = w < 2 ? (xcd * 4 + (j16 >> 2)) * 64 * 2048 + (j16 & 3) * 512 : w == 2 ? (j16 & 3) * 2048 : (xcd * 16 + j16) * 16;
    touch(r, L_DUM + (4 + w) * 256, lane == 0 ? 0u : OOB, so);
  }
  if (!KE) BAR();
  if (KV_SLEEP) __builtin_amdgcn_s_sleep(KV_SLEEP);
  const int cb = j16 & 3, t0 = (xcd * 4 + (j16 >> 2)) * 64, d0 = cb * 256 + w * 64;
  const int tg = lane >> 3, dc = lane & 7;
  const int tb = t0 + 8 * tg, dd = d0 + 8 * dc;
  u32x4 kr[11], vr[8], sh[4];
#pragma unroll
  for (int i = 3; i < 11; ++i) kr[i] = *(const u32x4*)(a.k + (size_t)(tb - 3 + i) * D + dd);
#pragma unroll
  for (int i = 0; i < 8; ++i) vr[i] = *(const u32x4*)(a.v + (size_t)(tb + i) * D + dd);
#pragma unroll
  for (int i = 0; i < 4; ++i) sh[i] = *(const u32x4*)(a.shk + (size_t)dd * 4 + 8 * i);
#pragma unroll
  for (int i = 0; i < 3; ++i) kr[i] = *(const u32x4*)(a.k + (size_t)(tb == 0 ? 0 : tb - 3 + i) * D + dd);
  PIN();
  if (KE) BAR();
  signal(c, E_KI);
  if (PFW == 1) warm1(a, c, xcd, j16);
  TAG_WAIT(c);
  if (tb == 0) {
#pragma unroll
    for (int i = 0; i < 3; ++i) kr[i] = u32x4{0u, 0u, 0u, 0u};
  }
  const u32 T = (w + 4) * CV_CH + 2 * CV_H;
#pragma unroll
  for (int e = 0; e < 4; ++e) {
    const f32x2 c0 = {bf_lo(sh[e][0]), bf_lo(sh[e][2])}, c1 = {bf_hi(sh[e][0]), bf_hi(sh[e][2])};
    const f32x2 c2 = {bf_lo(sh[e][1]), bf_lo(sh[e][3])}, c3 = {bf_hi(sh[e][1]), bf_hi(sh[e][3])};
    f32x2 kf[11], o[8];
#pragma unroll
    for (int i = 0; i < 11; ++i) kf[i] = bf_2(kr[i][e]);
#pragma unroll
    for (int i = 0; i < 8; ++i) o[i] = fma2(kf[i + 3], c0, fma2(kf[i + 2], c1, fma2(kf[i + 1], c2, kf[i] * c3))) * bf_2(vr[i][e]);
#pragma unroll
    for (int h = 0; h < 2; ++h)
      LDS(u32x4, T + (8 * dc + 2 * e + h) * 128 + (((7 - tg) ^ dc) * 16)) =
          u32x4{pack2(o[7][h], o[6][h]), pack2(o[5][h], o[4][h]), pack2(o[3][h], o[2][h]), pack2(o[1][h], o[0][h])};
  }
  STAMP(1);
#pragma unroll
  for (int q = 0; q < 8; ++q)
    st16(c.ws, tg * (S * 2) + dc * 16, WS_UR + (d0 + 8 * q) * (S * 2) + (S - 64 - t0) * 2, LDS(u32x4, T + (8 * q + tg) * 128 + ((dc ^ q) * 16)));
  PIN();
  P.init(c, xcd, j16 + 16);
  WAIT_VM(0);
  signal(c, E_KV);
  STAMP(2);
  if (w == 1) ln_rows(c, xcd, j16 + 16);
  if (w == 0) {
    await<1, 1>(E_KV);
    set_flag(c, F_KV + cb * 32 + xcd * 4 + (j16 >> 2));
    poll(c, lane < 32 ? foff(F_KV + (xcd >> 1) * 32 + lane) : OOB);
  }
  STAMP(3);
  STAMP_OUT(a.ws, 6);
}

#define CV_SLEEP 8
__device__ __forceinline__ void conv(const Args& a, const Ctx& c, int w8, int xcd, int j16) {
  STAMP_DECL; STAMP(0);
  const int lane = fresh(c.lane);
  const int u = xcd * 16 + j16, ch = u * 8 + w8;
  const u32 hb = w8 * CV_CH, ub = hb + 2 * CV_H;
  const float dcoef = bf_lo((u32)a.ssm_D[ch]);
  if (lane < 8) LDS(u32x4, ub + (lane >> 2) * CV_U + 4096 + (lane & 3) * 16) = u32x4{0u, 0u, 0u, 0u};
  WAIT_LGKM0();
  BAR();
#pragma unroll
  for (int i = 0; i < 4; ++i) {
    glds16<true>(c.ws, ub + CV_U + i * 1024, lane * 16, WS_UR + ch * (S * 2) + i * 1024);
    glds16<true>(c.ws, ub + i * 1024, lane * 16, WS_UR + ch * (S * 2) + i * 1024 + 4);
  }
  const int jc = j16 < 14 ? j16 : 13, rb = (xcd >> 2) * 2 + (jc & 1), cb = (xcd & 3) * 896 + (jc >> 1) * 128;
  WAIT_VM(0);
  if (lane == 0) LDS(u32, ub + 4092) = 0u;
  if (w8 >= 4) __builtin_amdgcn_s_sleep(CV_SLEEP);
  STAMP(1);
  const int r16 = lane & 15, g = lane >> 4;
  const u32 aaddr = hb + (r16 & 1) * CV_H + 64 * (r16 >> 1) + 16 * g;
  const u32 baddr = ub + (r16 & 1) * CV_U + 56 - 8 * (r16 >> 1) + 16 * g;
  f32x4 acc[8];
#pragma unroll
  for (int b = 0; b < 8; ++b) acc[b] = f32x4{0.f, 0.f, 0.f, 0.f};
  auto pass = [&](int r) {
    bf16x8 A[8], B[8];
#pragma unroll
    for (int p = 0; p < 8; ++p) {
      A[p] = LDS(bf16x8, aaddr + 64 * r + 512 * p);
      const u32x2 lo = LDS(volatile u32x2, baddr + 64 * r + 512 * (7 - p));
      const u32x2 hi = LDS(volatile u32x2, baddr + 64 * r + 512 * (7 - p) + 8);
      B[p] = __builtin_bit_cast(bf16x8, u32x4{lo[0], lo[1], hi[0], hi[1]});
    }
#pragma unroll
    for (int s = 0; s < 8; ++s)
#pragma unroll
      for (int p = 0; p <= s; ++p) acc[s] = __builtin_amdgcn_mfma_f32_16x16x32_bf16(A[p], B[s - p], acc[s], 0, 0, 0);
  };
#define W3R (3 + (w8 & 3))
  const int r0 = W3R;
#pragma unroll 1
  for (int r = 0; r < r0; ++r) {
    if (r == (T3R))
      touch(mk_rsrc(a.w3, D * F * 2), L_DUM + w8 * 256, (((lane >> 1) & 3) + 32 * (lane >> 3)) * (F * 2) + (lane & 1) * 128, (rb * 256 + 4 * w8) * (F * 2) + cb * 2);
    if (r == (T4R) && w8 < 7) touch(mk_rsrc(a.w4, F * D * 2), L_DUM + w8 * 256, lane * 128, (28 * u + 4 * w8) * 2048);
    pass(r);
  }
  PIN();
  u32x4 wr[8];
#pragma unroll
  for (int i = 0; i < 8; ++i) wr[i] = *(const u32x4*)(a.w3 + (size_t)(rb * 256 + 4 * w8 + (lane >> 4) + 32 * i) * F + cb + 8 * (lane & 15));
  PIN();
#pragma unroll 1
  for (int r = r0; r < 8; ++r) {
    pass(r);
  }
  STAMP(2);
  const u32 fo = (w8 == 4 && lane < 8) ? foff(F_X1 + ((xcd >> 2) * 8 + 4 + (j16 >> 2)) * 8 + lane) : OOB;
  u32x2 fv = peek(c, fo);
#pragma unroll
  for (int b = 0; b < 8; ++b) {
    const u32 o = hb + 512 * b + 4 * r16 + 128 * g;
    LDS(u32, o) = pack2(acc[b][0], acc[b][1]);
    LDS(u32, o + 64) = pack2(acc[b][2], acc[b][3]);
  }
#pragma unroll
  for (int i = 0; i < 4; ++i) {
    const int n = i * 64 + lane;
    const u32x4 y = LDS(u32x4, hb + n * 16);
    const u32x4 uu = LDS(u32x4, ub + CV_U + (255 - n) * 16);
    u32x4 o;
#pragma unroll
    for (int e = 0; e < 4; ++e)
      o[e] = pack2(__builtin_fmaf(bf_hi(uu[3 - e]), dcoef, bf_lo(y[e])), __builtin_fmaf(bf_lo(uu[3 - e]), dcoef, bf_hi(y[e])));
    st16(mk_rsrc(a.out, S * D * 2), (lane >> 3) * (64 * D * 2) + (lane & 7) * 16, (i * 512 + ((ch & 127) >> 1)) * (D * 2) + (ch >> 7) * 256 + (ch & 1) * 128, o);
  }
  PIN();
  {
    const u32 o01 = sconst(0x00003F80u), o10 = sconst(0x3F800000u);
    float cs[8];
#pragma unroll
    for (int e = 0; e < 8; ++e) {
      cs[e] = 0.f;
#pragma unroll
      for (int i = 0; i < 8; ++i) cs[e] = dot2(wr[i][e >> 1], (e & 1) ? o10 : o01, cs[e]);
    }
    const float u0 = red32(cs[0], cs[4]), u1 = red32(cs[1], cs[5]), u2 = red32(cs[2], cs[6]), u3 = red32(cs[3], cs[7]);
    LDS(f32x2, L_TB + w8 * 512 + r16 * 32 + g * 8) = f32x2{red16(u0, u2), red16(u1, u3)};
  }
  asm volatile("s_waitcnt vmcnt(4)" : "+v"(fv)::"memory");
  if (w8 == 4 && __builtin_amdgcn_ballot_w64(__builtin_bit_cast(u64, fv) != c.tag && fo != OOB) == 0) signal_all(E_XGO);
  WAIT_LGKM0();
  BAR();
  if (w8 < 2 && j16 < 14) {
    float t = 0.f;
#pragma unroll
    for (int i = 0; i < 8; ++i) t += LDS(float, L_TB + i * 512 + w8 * 256 + lane * 4);
    __builtin_amdgcn_raw_buffer_store_b32(__float_as_uint(t), c.ws, lane * 4, WS_C3 + (rb * F + cb + w8 * 64) * 4, 16);
  }
  STAMP(3);
  STAMP_OUT(a.ws, 3);
  asm volatile(".Lfused_last:");
}

template <int BN> struct WL;
template <> struct WL<128> { static constexpr int BYTES = 16 * 1056, HI = 256, KK = 8 * 1056; };
template <> struct WL<224> { static constexpr int BYTES = 4 * 7200, HI = 4 * 448, KK = 2 * 7200; };
constexpr int AP1 = 1088, AB1 = 16 * AP1, AB = 16384;
constexpr int ST1 = AB1 + WL<128>::BYTES, ST2 = AB + WL<128>::BYTES, ST3 = AB + WL<224>::BYTES;
static_assert(4 * ST1 <= (int)L_TB && 3 * ST3 <= (int)L_TB, "ring");

template <int NT> struct Frag { bf16x8 a[4]; s16x4 w[NT][2]; };

template <int BN, int MT, int AI>
__device__ __forceinline__ void rd(Frag<BN / 32>& f, int r, u32 ab, u32 wb) {
  constexpr int NT = BN / 32;
  if (r == 2) f.a[0] = LDS(bf16x8, ab);
  else if (r > 2 * NT) f.a[r - 2 * NT] = LDS(bf16x8, ab + (r - 2 * NT) * AI);
  else {
    const int z = r > 2 ? r - 1 : r;
    f.w[z >> 1][z & 1] = lds_tr(wb + (z >> 1) * 32 + (z & 1) * WL<BN>::HI);
  }
}
template <int BN, int MT, int AI, bool LOAD, class Late>
__device__ __forceinline__ void half(f32x4 (&acc)[4][BN / 32], const Frag<BN / 32>& c, Frag<BN / 32>& l, u32 ab, u32 wb, Late&& late) {
  constexpr int NT = BN / 32, NR = MT + 2 * NT, RPG = MT == 4 ? 1 : 2;
#pragma unroll
  for (int m = 0; m < MT * NT; ++m) {
    const int i = m / NT, j = m % NT;
    acc[i][j] = __builtin_amdgcn_mfma_f32_16x16x32_bf16(
        __builtin_bit_cast(bf16x8, __builtin_shufflevector(c.w[j][0], c.w[j][1], 0, 1, 2, 3, 4, 5, 6, 7)), c.a[i], acc[i][j], 0, 0, 0);
    PIN();
    if (LOAD) {
#pragma unroll
      for (int r = m * RPG; r < (m + 1) * RPG && r < NR; ++r) rd<BN, MT, AI>(l, r, ab, wb);
    }
    if (m == (NR + RPG - 1) / RPG) late();
    PIN();
  }
}
template <int BN, int MT, int AI, int AH, int STAGE, int NS, int MODE_>
__device__ __forceinline__ void step(f32x4 (&acc)[4][BN / 32], Frag<BN / 32>& f0, Frag<BN / 32>& f1, u32& ab, u32& wb, int& st, u32 ev = 0) {
  constexpr int MODE = MODE_ & 3;
  st = (st + 1 == NS) ? 0 : st + 1;
  const int dl = st == 0 ? -(NS - 1) * STAGE : STAGE;
  half<BN, MT, AI, true>(acc, f0, f1, ab + AH, wb + WL<BN>::KK, [&]() {
    ab += dl;
    wb += dl;
    asm volatile("" : "+v"(ab), "+v"(wb));
  });
  WAIT_LGKM0();
  if (MODE_ & 4) WAIT_VM(0);
  if (!(MODE & 1)) BAR();
  if (MODE == 1) LDS(volatile u32, ev) = 1u;
  PIN();
  half<BN, MT, AI, (MODE < 2)>(acc, f1, f0, ab, wb, [&]() {});
  if (MODE < 2) WAIT_LGKM0();
}

__device__ __forceinline__ u32 gelu2(f32x2 t, float c0, float c1, float sd) {
  const f32x2 z = t * fma2(t * t, dup2(c1), dup2(c0));
  const f32x2 e = {__builtin_amdgcn_exp2f(z[0]), __builtin_amdgcn_exp2f(z[1])};
  const f32x2 d = fma2(e, dup2(sd), dup2(sd));
  const f32x2 o = t * f32x2{__builtin_amdgcn_rcpf(d[0]), __builtin_amdgcn_rcpf(d[1])};
  return pack2(o[0], o[1]);
}
__device__ __forceinline__ u32x4 gelu16(f32x4 x, f32x4 y, f32x4 cs, f32x4 p, f32x4 q) {
  const u32 x01 = gelu2(fma2(lo2(cs), dup2(p[0]), lo2(x)), p[2], p[3], p[1]);
  const u32 x23 = gelu2(fma2(hi2(cs), dup2(p[0]), hi2(x)), p[2], p[3], p[1]);
  const u32 y01 = gelu2(fma2(lo2(cs), dup2(q[0]), lo2(y)), q[2], q[3], q[1]);
  const u32 y23 = gelu2(fma2(hi2(cs), dup2(q[0]), hi2(y)), q[2], q[3], q[1]);
  return swap16(x01, x23, y01, y23);
}
__device__ __forceinline__ f32x4 gelu_par(float nmean, float sd) {
  const float rs = __builtin_amdgcn_rcpf(sd);
  return f32x4{nmean, sd, -2.302208198f * rs, -0.1029432397f * rs * rs * rs};
}

struct Order {
  int cls, nxt;
  __device__ __forceinline__ Order(int c) : cls(c), nxt(c == 0 ? 1 : 0) {}
  __device__ __forceinline__ int operator()(int u) {
    if (u == 0) return cls;
    if (((u - 1) & 3) == cls && u + 3 < 16) return u + 3;
    const int kt = nxt;
    nxt += ((nxt + 1) & 3) == cls ? 2 : 1;
    return kt;
  }
};

struct Loader {
  rsrc_t rA, rW;
  u32 voA, voW, sa0, sa1, sw, la, lw, lb;
  int tab, n;
  __device__ __forceinline__ void W(int h, u32 k) {
    glds16<false>(rW, lb + lw + h * 8 * 1056, voW, sw + (k << 15));
    glds16<false>(rW, lb + lw + h * 8 * 1056 + 4 * 1056, voW, sw + (k << 15) + 8 * 2048);
  }
  template <bool BOTH>
  __device__ __forceinline__ void A(int h, u32 k) {
    glds16<true>(rA, lb + la + h * 8192, voA, sa0 + (k << 16));
    if (BOTH) glds16<true>(rA, lb + la + h * 8192 + 1024, voA, sa1 + (k << 16));
  }
  template <int M>
  __device__ __forceinline__ void issue() {
    const u32 k0 = __builtin_amdgcn_readlane(tab, n), k1 = __builtin_amdgcn_readlane(tab, n + 1);
    if (M & 2) A<!(M & 8)>(0, k0);
    if (M & 4) A<!(M & 8)>(1, k1);
    if (M & 1) {
      W(0, k0);
      W(1, k1);
    }
    n += 2;
    lb = (lb == 3 * ST2) ? 0 : lb + ST2;
  }
  __device__ __forceinline__ void seek(int stage) {
    n = 2 * stage;
    lb = (stage & 3) * ST2;
  }
};

struct Loader3 {
  rsrc_t rA, rW;
  u32 voA, voW[7], sa, sw, la, lw;
  __device__ __forceinline__ Loader3(const Args& a, const Ctx& c, int xcd, int idx, const Pre& P) {
    const int w = c.w;
    rA = c.ws;
    rW = mk_rsrc(a.w3, D * F * 2);
    voA = c.lane * 16;
#pragma unroll
    for (int i = 0; i < 7; ++i) voW[i] = P.voW[i];
    sa = WS_X1 + w * 65536 + ((xcd >> 2) * 8 + (idx >> 2)) * 4096;
    sw = (8 * (w & 1) + 32 * (w >> 1)) * (F * 2) + ((xcd & 3) * 4 + (idx & 3)) * 448;
    la = w * 4096;
    lw = AB + w * 7200;
  }
  __device__ __forceinline__ void W(u32 lb, int kt) {
#pragma unroll
    for (int i = 0; i < 7; ++i) glds16<false>(rW, lb + lw + i * 1024, voW[i], sw + kt * (64 * F * 2));
  }
  __device__ __forceinline__ void A(u32 lb, int kt) {
#pragma unroll
    for (int i = 0; i < 4; ++i) glds16<true>(rA, lb + la + i * 1024, voA, sa + kt * 262144 + i * 1024);
  }
};

__device__ __forceinline__ void load12(const Args& a, Ctx& c, int xcd, int idx, Pre& P) {
  STAMP_DECL; STAMP(0);
  TR_DECL;
  const int lane = c.lane, w = c.w;
  const int tm = (xcd >> 1) * 4 + (idx >> 2), tn = (xcd & 1) * 4 + (idx & 3);
  const int m0 = tm * 128, n0 = tn * 128;
  const u32 voW = (4 * ((lane >> 4) & 1) + 16 * (lane >> 5)) * (D * 2) + (lane & 15) * 16;
  Loader L;
  {
    const rsrc_t rA = mk_rsrc(a.x, S * D * 2), rW = mk_rsrc(a.w1, D * D * 2);
    const int sft = ((tn >> 2) + 1) * ((tn & 2) ? 1 : -1);
    u32 voA[4];
#pragma unroll
    for (int p = 0; p < 4; ++p) {
      const int r = m0 + sft + 32 * p + 8 * w + (lane >> 3);
      voA[p] = min(max(r, 0), S - 1) * (D * 2) + (((lane & 7) ^ (lane >> 4)) * 16);
    }
    Order ord(((idx >> 2) + idx) & 3);
    u32 lb = 0;
    auto issue = [&](int u) {
      const int kt = ord(u);
      const u32 sw = (kt * 64 + w) * (D * 2) + n0 * 2;
#pragma unroll
      for (int i = 0; i < 4; ++i) {
        glds16<false>(rW, lb + AB1 + (4 * i + w) * 1056, voW, sw + (8 * (i & 1) + 32 * (i >> 1)) * (D * 2));
        glds16<false>(rA, lb + (4 * i + w) * AP1, voA[i], kt * 128);
      }
      lb = (lb == 3 * ST1) ? 0 : lb + ST1;
    };
    issue(0);
    issue(1);
    LWAIT(8);
    LBAR();
    issue(2);
#pragma unroll 1
    for (int k = 0; k <= 12; ++k) {
      issue(k + 3);
      LWAIT(16);
      LBAR();
    }
    const u32 ro = WS_XS + min(max(min(m0 - 2 + 64 * w + lane, m0 + 129), 0), S - 1) * 16;
    const u32 co = WS_C1 + (tn * 4 + w) * 1024 + lane * 16;
    u32x4 rv = ald16<true>(c.ws, ro, 0), cv = ald16<true>(c.ws, co, 0);
    L.rA = c.ws;
    L.rW = mk_rsrc(a.w2, D * D * 2);
    L.voA = lane * 16;
    L.voW = voW;
    L.sa0 = WS_H + (w & 1) * 65536 + (m0 + (w >> 1) * 64) * 32;
    L.sa1 = L.sa0 + 1024;
    L.sw = w * (D * 2) + n0 * 2;
    L.la = (w & 1) * 4096 + (w >> 1) * 2048;
    L.lw = AB + w * 1056;
    {
      const int m = lane < 18 ? lane - 4 : lane - 18;
      L.tab = lane < 4 ? tn * 8 + 2 * lane : ((tn + 1 + (m >> 1)) & 7) * 8 + 4 * (m & 1) + (lane < 18 ? 0 : 2);
    }
    L.seek(0);
    L.issue<1>();
    LWAIT(14);
    LBAR();
    L.issue<1>();
    LWAIT(10);
    LBAR();
    L.issue<1>();
    TAG_WAIT(c);
    LWAIT(12);
    LANDED(rv);
    LANDED(cv);
#pragma unroll 1
    for (int n = 0; n < (1 << 20) && __builtin_amdgcn_ballot_w64(stale(c, rv) || stale(c, cv)) != 0; ++n) {
      rv = ald16<true>(c.ws, ro, 0);
      cv = ald16<true>(c.ws, co, 0);
      WAIT_VM(0);
      LANDED(rv);
      LANDED(cv);
    }
    if (w < 3) LDS(f32x4, L_TB + w * 1024 + lane * 16) = ln_par(__uint_as_float(rv[0]), __uint_as_float(rv[2]));
    const float t = red32(__uint_as_float(cv[0]), __uint_as_float(cv[2]));
    LDS(float, L_TB + 3072 + (32 * w + 2 * (lane & 15) + (lane >> 5)) * 4) = red16(t, t);
    WAIT_LGKM0();
    LBAR();
  }
  L.issue<1>();
  LWAIT(8);
  signal(c, E_2L);
  STAMP(1);
  await<1, 1>(E_HE);
  set_flag(c, F_HE + tm * 8 + tn, w == 0);
  P.init(c, xcd, idx);
  if (w == 1) ln_rows(c, xcd, idx);
  if (w == 0) {
    poll(c, lane < 8 ? foff(F_HE + tm * 8 + lane) : OOB);
    signal_all(E_HGO);
  } else {
    await<1, 1>(E_HGO);
  }
  STAMP(2);
  L.seek(2);
  L.issue<6>();
  L.issue<6>();
  await<1, 0>(E_2S);
  L.issue<7>();
  LWAIT(12);
  STAMP(3);
  LBAR();
  set_flag(c, F_HL + tm * 8 + tn, w == 0);
  L.issue<7>();
  LWAIT(17);
  LBAR();
  L.issue<7>();
  LWAIT(16);
  LBAR();
  const u32 fo = (w == 0 && lane < 8) ? foff(F_HL + tm * 8 + lane) : OOB;
  const u32x2 fv = peek(c, fo);
  L.issue<7>();
  LWAIT(17);
  LBAR();
  L.issue<7>();
  TRA();
  check<16>(c, fv, fo);
  LBAR();
#pragma unroll 1
  for (int k = 6; k <= 12; ++k) {
    L.issue<7>();
    LWAIT(16);
    LBAR();
  }
  Order ord(((idx >> 2) + idx) & 3);
  Loader3 L3(a, c, xcd, idx, P);
  LWAIT(8);
  LBAR();
  L3.W(0, ord(0));
  LWAIT(7);
  LBAR();
  L3.W(ST3, ord(1));
  LBAR();
  if (w == 0) {
    await<1, 1>(E_X1);
    set_flag(c, F_X1 + tm * 8 + tn);
  }
  STAMP_OUT(a.ws, 7);
  TR_OUT(a.ws, 0);
}

__device__ __forceinline__ void cons12(const Args& a, Ctx& c, int xcd, int idx) {
  const int lane = c.lane, w = c.w;
  const int tm = (xcd >> 1) * 4 + (idx >> 2), tn = (xcd & 1) * 4 + (idx & 3);
  const int m0 = tm * 128, n0 = tn * 128;
  const int wm = w >> 1, wn = w & 1, r16 = lane & 15, g = lane >> 4;
  const u32 wb0 = wn * 128 + 8 * (lane & 3) + ((r16 >> 2) + 4 * (g & 1)) * 1056 + (g >> 1) * 512;
  const u32 vo = (16 * (g & 1) + r16) * 32 + (g >> 1) * 16;
  f32x4 acc[4][4];
  Frag<4> f0, f1;
  u32x4 xr[4][2];
  {
    STAMP_DECL; STAMP(0);
    if (w == 0 && lane < E_N) LDS(u32x4, L_CNT + 16 * lane) = u32x4{0u, 0u, 0u, 0u};
    u32 ab = (wm * 8 + (r16 >> 3)) * AP1 + (r16 & 7) * 128 + ((g ^ ((r16 >> 1) & 3)) * 16), wb = AB1 + wb0;
#pragma unroll
    for (int i = 0; i < 4; ++i)
#pragma unroll
      for (int j = 0; j < 4; ++j) {
        acc[i][j] = f32x4{0.f, 0.f, 0.f, 0.f};
        asm volatile("" : "+v"(acc[i][j]));
      }
    TAG_WAIT(c);
    BAR();
    STAMP(1);
#pragma unroll
    for (int r = 0; r < 12; ++r) rd<128, 4, 2 * AP1>(f0, r, ab, wb);
    WAIT_LGKM0();
    PIN();
    int st = 0;
#pragma unroll 1
    for (int kt = 0; kt < 15; ++kt) step<128, 4, 2 * AP1, 64, ST1, 4, 0>(acc, f0, f1, ab, wb, st);
#pragma unroll
    for (int i = 0; i < 4; ++i)
#pragma unroll
      for (int j = 0; j < 2; ++j)
        xr[i][j] = *(const u32x4*)(a.x + (size_t)(m0 + wm * 64 + 16 * i + r16) * D + n0 + wn * 64 + 32 * j + 16 * (g & 1) + 8 * (g >> 1));
    PIN();
    step<128, 4, 2 * AP1, 64, ST1, 4, 2>(acc, f0, f1, ab, wb, st);
    STAMP(2);
    const int sft = ((tn >> 2) + 1) * ((tn & 2) ? 1 : -1);
    f32x4 p[4];
#pragma unroll
    for (int i = 0; i < 4; ++i) {
      f32x2 ms = LDS(f32x2, L_TB + (2 + sft + wm * 64 + 16 * i + r16) * 16 + 8);
      if ((i == 0 || i == 3) && (tm == 0 || tm == 15)) {
        const bool in = (u32)(m0 + sft + wm * 64 + 16 * i + r16) < (u32)S;
        ms = in ? ms : f32x2{0.f, 1.f};
#pragma unroll
        for (int j = 0; j < 4; ++j) acc[i][j] = in ? acc[i][j] : f32x4{0.f, 0.f, 0.f, 0.f};
      }
      p[i] = gelu_par(ms[0], ms[1]);
    }
#pragma unroll
    for (int j = 0; j < 4; ++j) {
      const f32x4 cs = LDS(f32x4, L_TB + 3072 + (wn * 64 + 16 * j + 4 * g) * 4);
#pragma unroll
      for (int ip = 0; ip < 2; ++ip) {
        const u32x4 v = gelu16(acc[2 * ip][j], acc[2 * ip + 1][j], cs, p[2 * ip], p[2 * ip + 1]);
        st16(c.ws, vo, WS_H + (tn * 8 + wn * 4 + j) * 65536 + (m0 + wm * 64 + ip * 32) * 32, v);
        LDS(u32x4, wn * ST2 + j * 4096 + (wm * 64 + ip * 32) * 32 + vo) = v;
      }
      if (j == 2) {
        WAIT_VM(2);
        signal(c, E_HE);
      }
    }
    STAMP(3);
    STAMP_OUT(a.ws, 1);
  }
  STAMP_DECL; STAMP(0);
  signal(c, E_2C);
#pragma unroll
  for (int i = 0; i < 4; ++i) {
    const f32x2 xs = LDS(f32x2, L_TB + (2 + wm * 64 + 16 * i + r16) * 16);
#pragma unroll
    for (int j = 0; j < 2; ++j) {
      const u32x4 xv = swap16(xr[i][j][0], xr[i][j][1], xr[i][j][2], xr[i][j][3]);
#pragma unroll
      for (int h = 0; h < 2; ++h) {
        const f32x2 l = fma2(bf_2(xv[2 * h]), dup2(xs[0]), dup2(xs[1])), u = fma2(bf_2(xv[2 * h + 1]), dup2(xs[0]), dup2(xs[1]));
        acc[i][2 * j + h] = f32x4{l[0], l[1], u[0], u[1]};
        asm volatile("" : "+v"(acc[i][2 * j + h]));
      }
    }
  }
  u32 ab = (g >> 1) * 4096 + (wm * 64 + r16) * 32 + (g & 1) * 16, wb = AB + wb0;
  await<2, 0>(E_2C);
  STAMP(1);
#pragma unroll
  for (int r = 0; r < 12; ++r) rd<128, 4, 512>(f0, r, ab, wb);
  WAIT_LGKM0();
  PIN();
  int st = 0;
  step<128, 4, 512, 8192, ST2, 4, 1>(acc, f0, f1, ab, wb, st, L_CNT + 16 * E_2S + 4 * w);
#pragma unroll 1
  for (int kt = 1; kt < 15; ++kt) step<128, 4, 512, 8192, ST2, 4, 4>(acc, f0, f1, ab, wb, st);
  step<128, 4, 512, 8192, ST2, 4, 2>(acc, f0, f1, ab, wb, st);
  STAMP(2);
#pragma unroll
  for (int j = 0; j < 4; ++j)
#pragma unroll
    for (int ip = 0; ip < 2; ++ip) {
      const f32x4 x = acc[2 * ip][j], y = acc[2 * ip + 1][j];
      st16(c.ws, vo, WS_X1 + (tn * 8 + wn * 4 + j) * 65536 + (m0 + wm * 64 + ip * 32) * 32,
           swap16(pack2(x[0], x[1]), pack2(x[2], x[3]), pack2(y[0], y[1]), pack2(y[2], y[3])));
    }
  PIN();
  float t1[4], t2[4];
#pragma unroll
  for (int i = 0; i < 4; ++i) {
    f32x2 s1 = lo2(acc[i][0]) + hi2(acc[i][0]), s2 = fma2(lo2(acc[i][0]), lo2(acc[i][0]), hi2(acc[i][0]) * hi2(acc[i][0]));
#pragma unroll
    for (int j = 1; j < 4; ++j) {
      s1 += lo2(acc[i][j]) + hi2(acc[i][j]);
      s2 = fma2(lo2(acc[i][j]), lo2(acc[i][j]), fma2(hi2(acc[i][j]), hi2(acc[i][j]), s2));
    }
    t1[i] = s1[0] + s1[1];
    t2[i] = s2[0] + s2[1];
  }
  const float r1 = red16(red32(t1[0], t1[2]), red32(t1[1], t1[3])), r2 = red16(red32(t2[0], t2[2]), red32(t2[1], t2[3]));
  __builtin_amdgcn_raw_buffer_store_b64(u32x2{__float_as_uint(r1), __float_as_uint(r2)}, c.ws, lane * 8,
                                        WS_ST + ((tm * 16 + tn * 2 + wn) * 128 + wm * 64) * 8, 16);
  WAIT_VM(1);
  signal(c, E_X1);
  STAMP(3);
  STAMP_OUT(a.ws, 2);
}

#define GEJ 3
#define TA 2
#define TB 5
__device__ __forceinline__ void load34(const Args& a, const Ctx& c, int xcd, int idx, const Pre& P) {
  TR_DECL;
  asm volatile("" : "+s"(xcd), "+s"(idx));
  const int lane = fresh(c.lane), w = c.w;
  const int tm = (xcd >> 2) * 8 + (idx >> 2), m0 = tm * 128, cls = ((idx >> 2) + idx) & 3;
  const int blk = (xcd & 1) * 4 + (idx & 3), p = (xcd >> 1) & 1, n0 = blk * 128;
  Loader L;
  L.rW = mk_rsrc(a.w4, F * D * 2);
  {
    Loader3 L3(a, c, xcd, idx, P);
    Order ord(cls);
    u32 lb = 0;
    auto issue = [&](int u) {
      const int kt = ord(u);
      L3.A(lb, kt);
      L3.W(lb, kt);
      lb = (lb == 2 * ST3) ? 0 : lb + ST3;
    };
    const bool fast = __builtin_amdgcn_readfirstlane(LDS(volatile u32, L_CNT + 16 * E_XGO + 4 * w)) && idx >= 16;
    if (fast) {
#pragma unroll 1
      for (int u = 0; u < 3; ++u) issue(u);
    } else {
      const int k0 = ord(0), k1 = ord(1), k2 = ord(2);
      if (idx >= 16) {
        L3.W(0, k0);
        L3.W(ST3, k1);
      }
      if (w == 0) {
        poll(c, lane < 8 ? foff(F_X1 + tm * 8 + lane) : OOB);
        signal_all(E_XGO);
      } else {
        await<1, 1>(E_XGO);
      }
      L3.A(0, k0);
      L3.A(ST3, k1);
      L3.W(2 * ST3, k2);
      L3.A(2 * ST3, k2);
    }
    LWAIT(22);
    if (!fast) WAIT_VM(15);
    LBAR();
    set_flag(c, idx < 16 ? F_ST + ((xcd >> 1) * 4 + (idx >> 2)) * 8 + blk : F_C + xcd * 16 + idx - 16, w == 0);
    L.rA = c.ws;
    L.voA = L3.voA;
    L.voW = P.voW4;
    L.sa0 = WS_G3 + (w & 1) * 65536 + (m0 + p * 64 + (w >> 1) * 32) * 32;
    L.sa1 = L.sa0 ^ 2048;
    L.sw = w * (D * 2) + n0 * 2;
    L.la = (w & 1) * 4096 + (w >> 1) * 2048;
    L.lw = AB + w * 1056;
    L.tab = P.tab;
    LWAIT(12);
    LBAR();
    const u32 syo = (m0 + p * 64 + w * 16) * (D * 2) + n0 * 2, sco = WS_C3 + ((xcd & 3) * 4 + (idx & 3)) * 896;
#pragma unroll 1
    for (int u = 3; u <= 8; ++u) {
      issue(u);
      LWAIT(11);
      LBAR();
    }
    const u32x2 fv = peek(c, P.fo);
    issue(9);
    LWAIT(12);
    LBAR();
    issue(10);
    TRA();
    check<11>(c, fv, P.fo);
    LBAR();
    u32x4 sv[4];
    u32 cv[4];
#pragma unroll
    for (int k = 0; k < 2; ++k) {
#pragma unroll
      for (int i = 2 * k; i < 2 * k + 2; ++i) {
        sv[i] = ald16<true>(c.ws, P.os, WS_ST + (tm * 16 + i) * 1024 + w * 256);
        cv[i] = ald4(c.ws, P.oc, sco + i * (F * 4));
      }
      issue(11 + k);
      LWAIT(15);
      LBAR();
    }
    issue(13);
    LWAIT(11);
    LBAR();
    issue(14);
#pragma unroll
    for (int i = 0; i < 4; ++i) {
      LANDED(sv[i]);
      LANDED(cv[i]);
    }
    f32x4 t = (__builtin_bit_cast(f32x4, sv[0]) + __builtin_bit_cast(f32x4, sv[1])) + (__builtin_bit_cast(f32x4, sv[2]) + __builtin_bit_cast(f32x4, sv[3]));
    LDS(float, L_T3 + 2048 + min(w * 64 + lane, 223) * 4) = (__uint_as_float(cv[0]) + __uint_as_float(cv[1])) + (__uint_as_float(cv[2]) + __uint_as_float(cv[3]));
    LANDED(t);
    LWAIT(11);
    LBAR();
    issue(15);
    float s1 = red32(t[0], t[2]), s2 = red32(t[1], t[3]);
    s1 = red16(s1, s1);
    s2 = red16(s2, s2);
    float mean = s1 * (1.0f / D);
    float var = fmaxf(s2 * (1.0f / D) - mean * mean, 0.f);
    LANDED(mean);
    LANDED(var);
    LWAIT(11);
    LBAR();
    L.seek(1);
    L.issue<1>();
    LDS(f32x4, L_T3 + (32 * w + 2 * (lane & 15) + (lane >> 5)) * 16) = gelu_par(-rbf(mean), rbf(__builtin_amdgcn_sqrtf(rbf(rbf(var) + 1e-5f))));
    PIN();
    LWAIT(4);
    LBAR();
    L.issue<1>();
    L.issue<1>();
    WAIT_LGKM0();
    LBAR();
    L.seek(0);
    L.issue<1>();
#pragma unroll
    for (int i = 0; i < 4; ++i) glds16<true>(mk_rsrc(a.out, S * D * 2), L_TB + w * 4096 + i * 1024, P.oy, syo + i * (4 * D * 2));
  }
  LWAIT(0);
  signal(c, E_4L);
  if (w == 0) {
    await<1, 1>(E_GE);
    set_flag(c, F_GE + tm * 16 + p * 8 + blk);
    poll(c, lane < 8 ? foff(F_GE + tm * 16 + p * 8 + lane) : OOB);
    signal_all(E_GGO);
  } else {
    await<1, 1>(E_GGO);
  }
  L.seek(3);
  L.issue<4>();
  await<1, 1>(E_4S0);
  L.issue<7>();
  await<1, 0>(E_4S1);
  L.issue<7>();
  LWAIT(16);
  LBAR();
  set_flag(c, F_GL + tm * 16 + p * 8 + blk, w == 0);
  L.issue<7>();
  LWAIT(17);
  LBAR();
  L.issue<7>();
  LWAIT(16);
  LBAR();
#pragma unroll 1
  for (int h = 0; h < 2; ++h) {
    const u32 fo = (w == 0 && lane < 8) ? foff(F_GL + tm * 16 + (p ^ h) * 8 + lane) : OOB;
    const u32x2 fv = peek(c, fo);
    L.issue<7>();
    LWAIT(17);
    LBAR();
    L.issue<7>();
    TRA();
    check<16>(c, fv, fo);
    LBAR();
    if (h) break;
#pragma unroll 1
    for (int k = 7; k <= NFS - 6; ++k) {
      L.issue<7>();
      LWAIT(16);
      LBAR();
    }
  }
  L.issue<15>();
  LWAIT(14);
  LBAR();
#pragma unroll 1
  for (int k = 1; k < 4; ++k) {
    L.issue<15>();
    LWAIT(12);
    LBAR();
  }
  const u32 fo = (w == 0 && lane == 0) ? foff(F_EX + (blockIdx.x ^ 2)) : OOB;
  u32x2 fv;
#pragma unroll
  for (int k = 1; k < TAIL - 1; ++k) {
    if (k == TA + 1) set_flag(c, F_EX + blockIdx.x, w == 0);
    if (k == TB - 1) fv = peek(c, fo);
    if (k + 3 < TAIL) L.issue<15>();
    const int n = (k + 2 < TAIL ? 6 : 0) + (k + 3 < TAIL ? 6 : 0);
    if (k == TB) {
      TRA();
      check<(TB + 2 < TAIL ? 6 : 0) + (TB + 3 < TAIL ? 6 : 0)>(c, fv, fo);
    } else {
      LWAIT(n + (k == TA + 1 || k == TA + 2) + (k == TB - 1));
    }
    LBAR();
  }
  if (blockIdx.x == 0 && w == 0) {
#pragma unroll 1
    for (int i = 0; i < 4; ++i) poll(c, foff(F_GL + i * 64 + lane));
    if (lane == 0) __hip_atomic_store((gu64*)a.ws, c.tag, __ATOMIC_RELAXED, __HIP_MEMORY_SCOPE_AGENT);
  }
  TR_OUT(a.ws, 1);
}

__device__ __forceinline__ void cons34(const Args& a, const Ctx& c, int xcd, int idx) {
  const int lane = fresh(c.lane), w = c.w;
  const int tm = (xcd >> 2) * 8 + (idx >> 2), m0 = tm * 128;
  const int wm = w >> 1, wn = w & 1, r16 = lane & 15, g = lane >> 4;
  const int blk = (xcd & 1) * 4 + (idx & 3), p = (xcd >> 1) & 1, n0 = blk * 128;
  const int row0 = m0 + p * 64 + wm * 32;
  const u32 vo = (16 * (g & 1) + r16) * 32 + (g >> 1) * 16;
  const u32 ab0 = (g >> 1) * 4096 + (wm * 64 + r16) * 32 + (g & 1) * 16;
  u32x4 x1r[4], xv[2][2];
  {
    STAMP_DECL; STAMP(0);
    const int tn = (xcd & 3) * 4 + (idx & 3);
    f32x4 acc[4][7];
    Frag<7> f0, f1;
    u32 ab = ab0, wb = AB + wn * 224 + 8 * (lane & 3) + (g & 1) * 7200 + ((r16 >> 2) + 8 * (g >> 1)) * 448;
#pragma unroll
    for (int i = 0; i < 4; ++i)
#pragma unroll
      for (int j = 0; j < 7; ++j) {
        acc[i][j] = f32x4{0.f, 0.f, 0.f, 0.f};
        asm volatile("" : "+v"(acc[i][j]));
      }
    WAIT_VM(0);
    BAR();
    STAMP(1);
#pragma unroll
    for (int r = 0; r < 18; ++r) rd<224, 4, 512>(f0, r, ab, wb);
    WAIT_LGKM0();
    PIN();
    int st = 0;
#pragma unroll 1
    for (int kt = 0; kt < 15; ++kt) step<224, 4, 512, 8192, ST3, 3, 0>(acc, f0, f1, ab, wb, st);
    step<224, 4, 512, 8192, ST3, 3, 2>(acc, f0, f1, ab, wb, st);
    STAMP(2);
#pragma unroll
    for (int j = 0; j < 4; ++j) {
      x1r[j] = ld16(c.ws, vo, WS_X1 + (blk * 8 + wn * 4 + j) * 65536 + row0 * 32);
      xv[j >> 1][j & 1] = *(const u32x4*)(a.x + (size_t)(row0 + 16 * (j >> 1) + r16) * D + n0 + wn * 64 + 32 * (j & 1) + 16 * (g & 1) + 8 * (g >> 1));
    }
    PIN();
    f32x4 q[4];
#pragma unroll
    for (int i = 0; i < 4; ++i) q[i] = LDS(f32x4, L_T3 + (wm * 64 + 16 * i + r16) * 16);
#pragma unroll
    for (int j = 0; j < 7; ++j) {
      const f32x4 cs = LDS(f32x4, L_T3 + 2048 + (wn * 112 + 16 * j + 4 * g) * 4);
      const int kg = j < 6 ? wn * 6 + j : 12 + wn;
#pragma unroll
      for (int ip = 0; ip < 2; ++ip) {
        const u32x4 v = gelu16(acc[2 * ip][j], acc[2 * ip + 1][j], cs, q[2 * ip], q[2 * ip + 1]);
        st16(c.ws, vo, WS_G3 + (tn * 14 + kg) * 65536 + (m0 + wm * 64 + ip * 32) * 32, v);
        LDS(u32x4, (kg >> 2) * ST2 + (kg & 3) * 4096 + (2 * ip + (wm ^ p)) * 1024 + vo) = v;
      }
      if (j == GEJ) {
        WAIT_VM(2 * GEJ - 2);
        signal(c, E_GE);
      }
    }
    STAMP(3);
    STAMP_OUT(a.ws, 4);
  }
  STAMP_DECL; STAMP(0);
  signal(c, E_4C);
  f32x4 acc[4][4];
  Frag<4> f0, f1;
#pragma unroll
  for (int j = 0; j < 4; ++j) {
    const u32x4 x1 = swap16(x1r[j][0], x1r[j][1], x1r[j][2], x1r[j][3]);
#pragma unroll
    for (int i = 0; i < 4; ++i) {
      acc[i][j] = i < 2 ? f32x4{bf_lo(x1[2 * i]), bf_hi(x1[2 * i]), bf_lo(x1[2 * i + 1]), bf_hi(x1[2 * i + 1])} : f32x4{0.f, 0.f, 0.f, 0.f};
      asm volatile("" : "+v"(acc[i][j]));
    }
  }
  u32 ab = ab0, wb = AB + wn * 128 + 8 * (lane & 3) + ((r16 >> 2) + 4 * (g & 1)) * 1056 + (g >> 1) * 512;
  await<2, 0>(E_4C);
  STAMP(1);
#pragma unroll
  for (int r = 0; r < 12; ++r) rd<128, 4, 512>(f0, r, ab, wb);
  WAIT_LGKM0();
  PIN();
  int st = 0;
#pragma unroll 1
  for (int kt = 0; kt < 2; ++kt) step<128, 4, 512, 8192, ST2, 4, 1>(acc, f0, f1, ab, wb, st, L_CNT + 16 * (E_4S0 + kt) + 4 * w);
  f32x4 sv[2][4];
#pragma unroll
  for (int i = 0; i < 2; ++i) {
    const f32x2 xs = LDS(f32x2, L_LN + (wm * 32 + 16 * i + r16) * 8);
#pragma unroll
    for (int j = 0; j < 4; ++j) {
      if (!(j & 1)) xv[i][j / 2] = swap16(xv[i][j / 2][0], xv[i][j / 2][1], xv[i][j / 2][2], xv[i][j / 2][3]);
      const int dr = wn * 64 + 16 * j + 4 * g + (r16 >> 2);
      const u32x2 yv = __builtin_bit_cast(u32x2, lds_tr(L_TB + dr * 128 + (((2 * wm + i) ^ (dr >> 1 & 3)) * 32) + 8 * (lane & 3)));
      const f32x2 l = fma2(bf_2(xv[i][j / 2][2 * (j & 1)]), dup2(xs[0]), dup2(xs[1])) * bf_2(yv[0]);
      const f32x2 u = fma2(bf_2(xv[i][j / 2][2 * (j & 1) + 1]), dup2(xs[0]), dup2(xs[1])) * bf_2(yv[1]);
      sv[i][j] = f32x4{l[0], l[1], u[0], u[1]};
      asm volatile("" : "+v"(sv[i][j]), "+v"(xv[i][j / 2]));
    }
  }
  PIN();
#pragma unroll 1
  for (int kt = 2; kt < NFS; ++kt) step<128, 4, 512, 8192, ST2, 4, 4>(acc, f0, f1, ab, wb, st);
#pragma unroll
  for (int j = 0; j < 4; ++j)
    st16(c.ws, lane * 16, WS_X1 + ((p * 8 + blk) * 4 + w) * 65536 + tm * 4096 + j * 1024,
         u32x4{pack2(acc[2][j][0], acc[2][j][1]), pack2(acc[2][j][2], acc[2][j][3]), pack2(acc[3][j][0], acc[3][j][1]), pack2(acc[3][j][2], acc[3][j][3])});
  PIN();
#pragma unroll 1
  for (int k = 0; k < TA; ++k) step<128, 2, 512, 8192, ST2, 4, 0>(acc, f0, f1, ab, wb, st);
#pragma unroll 1
  for (int k = TA; k <= TB; ++k) step<128, 2, 512, 8192, ST2, 4, 4>(acc, f0, f1, ab, wb, st);
  u32x4 ex[4];
#pragma unroll
  for (int j = 0; j < 4; ++j) ex[j] = ald16<true>(c.ws, lane * 16, WS_X1 + (((p ^ 1) * 8 + blk) * 4 + w) * 65536 + tm * 4096 + j * 1024);
  PIN();
#pragma unroll 1
  for (int k = TB + 1; k < TAIL - 1; ++k) step<128, 2, 512, 8192, ST2, 4, 0>(acc, f0, f1, ab, wb, st);
  step<128, 2, 512, 8192, ST2, 4, 3>(acc, f0, f1, ab, wb, st);
  STAMP(2);
  asm volatile("s_waitcnt vmcnt(0)" : "+v"(ex[0]), "+v"(ex[1]), "+v"(ex[2]), "+v"(ex[3])::"memory");
#pragma unroll
  for (int i = 0; i < 2; ++i)
#pragma unroll
    for (int jp = 0; jp < 2; ++jp) {
      u32 o[4];
#pragma unroll
      for (int e = 0; e < 4; ++e) {
        const int j = 2 * jp + (e >> 1);
        const f32x2 ob = ((e & 1) ? hi2(acc[i][j]) : lo2(acc[i][j])) + bf_2(ex[j][2 * i + (e & 1)]);
        const f32x2 z = ob * dup2(-1.442695041f);
        const f32x2 d = f32x2{__builtin_amdgcn_exp2f(z[0]), __builtin_amdgcn_exp2f(z[1])} + dup2(1.0f);
        const f32x2 gt = ob * f32x2{__builtin_amdgcn_rcpf(d[0]), __builtin_amdgcn_rcpf(d[1])};
        const f32x2 r = fma2((e & 1) ? hi2(sv[i][j]) : lo2(sv[i][j]), gt, bf_2(xv[i][jp][e]));
        o[e] = pack2(r[0], r[1]);
      }
      *(u32x4*)(a.out + (size_t)(row0 + 16 * i + r16) * D + n0 + wn * 64 + 32 * jp + 16 * (g & 1) + 8 * (g >> 1)) = swap16(o[0], o[1], o[2], o[3]);
    }
  STAMP(3);
  STAMP_OUT(a.ws, 5);
}

__global__ __launch_bounds__(512, 2) void fused(Args a) {
  const int xcd = blockIdx.x & 7, idx = blockIdx.x >> 3;
  const int w8 = __builtin_amdgcn_readfirstlane(threadIdx.x >> 6);
  Ctx c;
  c.tag = *(const __attribute__((address_space(4))) u64*)a.ws;
  c.ws = mk_rsrc(a.ws, WS_END);
  c.lane = threadIdx.x & 63;
  c.w = w8 & 3;
  Pre P;
  if (idx >= 16 && w8 == 0) {
    u64 pc;
    u32 len;
    asm volatile("s_getpc_b64 %0\n.Lfused_first:\n\ts_mov_b32 %1, .Lfused_last-.Lfused_first" : "=s"(pc), "=s"(len));
    touch(mk_rsrc((const void*)pc, len), L_DUM, c.lane < 40 ? c.lane * 128 + (idx - 16) * 5120 : OOB, 0);
  }
  if (__builtin_expect(idx < 16, 1)) {
    if (__builtin_expect(w8 >= 4, 1)) load12(a, c, xcd, idx, P);
    else cons12(a, c, xcd, idx);
  } else {
    if (w8 >= 4) side_kv(a, c, xcd, idx - 16, P);
    else side_stats(a, c, xcd, idx - 16);
    conv(a, c, w8, xcd, idx - 16);
  }
  if (w8 >= 4) load34(a, c, xcd, idx, P);
  else cons34(a, c, xcd, idx);
}

extern "C" void kernel_launch(void* const* d_in, const int* in_sizes, int n_in, void* d_out, int out_size, void* d_ws,
                              size_t ws_size, hipStream_t stream) {
  Args a;
  a.x = (const u16*)d_in[0];
  a.k = (const u16*)d_in[1];
  a.v = (const u16*)d_in[2];
  a.shk = (const u16*)d_in[3];
  a.ssm_k = (const u16*)d_in[4];
  a.ssm_D = (const u16*)d_in[5];
  a.w1 = (const u16*)d_in[7];
  a.w2 = (const u16*)d_in[8];
  a.w3 = (const u16*)d_in[9];
  a.w4 = (const u16*)d_in[10];
  a.ws = (char*)d_ws;
  a.out = (u16*)d_out;
  (void)hipFuncSetAttribute((const void*)fused, hipFuncAttributeMaxDynamicSharedMemorySize, LDS_BYTES);
  fused<<<256, 512, LDS_BYTES, stream>>>(a);
}
```
